```python
import torch

def _softmax(x, axis=-1):
    return torch.softmax(x, dim=axis)
def _gelu(x):
    return torch.nn.functional.gelu(x, approximate="tanh")
def _silu(x):
    return torch.nn.functional.silu(x)
def _sigmoid(x):
    return torch.sigmoid(x)
def _layernorm(x, axis=-1, eps=1e-5):
    m = x.mean(dim=axis, keepdim=True)
    v = x.var(dim=axis, keepdim=True, unbiased=False)
    return (x - m) / torch.sqrt(v + eps)
def _scatter_add(tgt, idx, src, axis=0):
    return tgt.clone().index_add_(axis, idx, src)
def _segment_sum(src, seg, n):
    out = torch.zeros((n,) + src.shape[1:], dtype=src.dtype)
    return out.index_add_(0, seg, src)
def _pad(x, pw):
    flat = tuple(v for pair in reversed(pw) for v in pair)
    return torch.nn.functional.pad(x, flat)

kernel_name = 'recurrent_scan__mha_block_cached__geglu_ffn__prompt'

def setup_inputs():

    _w = (lambda a: (lambda t: t.to(torch.bfloat16) if t.is_floating_point() else t)(torch.as_tensor(a)))
    def _A_setup():
        B, Sq, D = 1, 2048, 576
        H, Sk, Dh = 32, 2048, 128
        return {
            "x": _w((lambda *s: torch.randn(*s, dtype=torch.bfloat16) * 0.05)(B, Sq, D)),
            "k_cache": _w((lambda *s: torch.randn(*s, dtype=torch.bfloat16) * 0.8)(B, H, Sk, Dh)),
            "v_cache": _w((lambda *s: torch.randn(*s, dtype=torch.bfloat16) * 0.2)(B, H, Sk, Dh)),
            "wq": _w((lambda *s: torch.randn(*s, dtype=torch.bfloat16) * 0.8)(D, H * Dh) * (D ** -0.5)),
            "wo": _w((lambda *s: torch.randn(*s, dtype=torch.bfloat16) * 0.2)(H * Dh, D) * ((H * Dh) ** -0.5)),
        }
    _A_in = _A_setup()

    def _B_setup():
        B, S, D, F = 1, 2048, 576, 4608
        return {
            "x": _w((lambda *s: torch.randn(*s, dtype=torch.bfloat16) * 0.05)(B, S, D)),
            "w_gate": _w((lambda *s: torch.randn(*s, dtype=torch.bfloat16) * 0.1)(D, F)),
            "w_up": _w((lambda *s: torch.randn(*s, dtype=torch.bfloat16) * 0.1)(D, F)),
            "w_down": _w((lambda *s: torch.randn(*s, dtype=torch.bfloat16) * 0.1)(F, D)),
        }
    _B_in = _B_setup()

    return {
        "A_x": _A_in["x"],
        "A_k_cache": _A_in["k_cache"],
        "A_v_cache": _A_in["v_cache"],
        "A_wq": _A_in["wq"],
        "A_wo": _A_in["wo"],
        "B_x": _B_in["x"],
        "B_w_gate": _B_in["w_gate"],
        "B_w_up": _B_in["w_up"],
        "B_w_down": _B_in["w_down"],
    }

def reference(A_x, A_k_cache, A_v_cache, A_wq, A_wo, B_x, B_w_gate, B_w_up, B_w_down):
    def _A_ref(x, k_cache, v_cache, wq, wo):
        B, Sq, D = x.shape
        H, Dh = 32, 128
        q = (lambda x, *d: x.permute(*d))((x @ wq).reshape(B, Sq, H, Dh), 0, 2, 1, 3)
        scores = torch.einsum("bhqd,bhkd->bhqk", q, k_cache) * (Dh ** -0.5)
        p = _softmax(scores, axis=-1)
        o = torch.einsum("bhqk,bhkd->bhqd", p, v_cache)
        return (lambda x, *d: x.permute(*d))(o, 0, 2, 1, 3).reshape(B, Sq, H * Dh) @ wo

    def _B_ref(x, w_gate, w_up, w_down):
        return (_gelu(x @ w_gate) * (x @ w_up)) @ w_down

    x = A_x
    xn = _layernorm(x, axis=-1)
    s = _A_ref(xn, A_k_cache, A_v_cache, A_wq, A_wo)
    g = _silu(_B_ref(xn, B_w_gate, B_w_up, B_w_down))
    return x + s * g
```

```cpp
#include <hip/hip_runtime.h>
#include <hip/hip_bf16.h>
#include <utility>

constexpr int SEQ = 2048, DM = 576, NH = 32, FF = 4608, HD = 128;
constexpr int NT = SEQ / 64;
constexpr float THR = 8.0f;
constexpr unsigned KRS = 256u, KTILEB = 64u * KRS;
typedef float    f32x16 __attribute__((ext_vector_type(16)));
typedef float    f32x2  __attribute__((ext_vector_type(2)));
typedef float    f32x4  __attribute__((ext_vector_type(4)));
typedef __bf16   bf16x8 __attribute__((ext_vector_type(8)));
typedef __bf16   bf16x4 __attribute__((ext_vector_type(4)));
typedef __bf16   bf16x2 __attribute__((ext_vector_type(2)));
typedef unsigned u32x4  __attribute__((ext_vector_type(4)));
typedef unsigned u32x2  __attribute__((ext_vector_type(2)));
template <int... Is> using iseq = std::integer_sequence<int, Is...>;
template <int N> using make_iseq = std::make_integer_sequence<int, N>;
#define KRING 0x0000u
#define VRING 0x8000u
#define SRD_WORD3 0x00020000
#define DI __device__ __forceinline__
#define GAP() __builtin_amdgcn_sched_barrier(0)
#define W4_ACLOB_HI "a128","a129","a130","a131","a132","a133","a134","a135","a136","a137","a138","a139","a140","a141","a142","a143","a144","a145","a146","a147","a148","a149","a150","a151","a152","a153","a154","a155","a156","a157","a158","a159","a160","a161","a162","a163","a164","a165","a166","a167","a168","a169","a170","a171","a172","a173","a174","a175","a176","a177","a178","a179","a180","a181","a182","a183","a184","a185","a186","a187","a188","a189","a190","a191","a192","a193","a194","a195","a196","a197","a198","a199","a200","a201","a202","a203","a204","a205","a206","a207","a208","a209","a210","a211","a212","a213","a214","a215","a216","a217","a218","a219","a220","a221","a222","a223","a224","a225","a226","a227","a228","a229","a230","a231","a232","a233","a234","a235","a236","a237","a238","a239","a240","a241","a242","a243","a244","a245","a246","a247","a248","a249","a250","a251","a252","a253","a254","a255"
#define W4_ACLOB "a0","a1","a2","a3","a4","a5","a6","a7","a8","a9","a10","a11","a12","a13","a14","a15","a16","a17","a18","a19","a20","a21","a22","a23","a24","a25","a26","a27","a28","a29","a30","a31","a32","a33","a34","a35","a36","a37","a38","a39","a40","a41","a42","a43","a44","a45","a46","a47","a48","a49","a50","a51","a52","a53","a54","a55","a56","a57","a58","a59","a60","a61","a62","a63","a64","a65","a66","a67","a68","a69","a70","a71","a72","a73","a74","a75","a76","a77","a78","a79","a80","a81","a82","a83","a84","a85","a86","a87","a88","a89","a90","a91","a92","a93","a94","a95","a96","a97","a98","a99","a100","a101","a102","a103","a104","a105","a106","a107","a108","a109","a110","a111","a112","a113","a114","a115","a116","a117","a118","a119","a120","a121","a122","a123","a124","a125","a126","a127"
#define W4_READ_O(DB, QB, o) do { switch (2 * (DB) + (QB)) { \
    case 0: asm volatile("v_accvgpr_read_b32 %0, a0\n\t" "v_accvgpr_read_b32 %1, a1\n\t" "v_accvgpr_read_b32 %2, a2\n\t" "v_accvgpr_read_b32 %3, a3\n\t" "v_accvgpr_read_b32 %4, a4\n\t" "v_accvgpr_read_b32 %5, a5\n\t" "v_accvgpr_read_b32 %6, a6\n\t" "v_accvgpr_read_b32 %7, a7\n\t" "v_accvgpr_read_b32 %8, a8\n\t" "v_accvgpr_read_b32 %9, a9\n\t" "v_accvgpr_read_b32 %10, a10\n\t" "v_accvgpr_read_b32 %11, a11\n\t" "v_accvgpr_read_b32 %12, a12\n\t" "v_accvgpr_read_b32 %13, a13\n\t" "v_accvgpr_read_b32 %14, a14\n\t" "v_accvgpr_read_b32 %15, a15\n\t" : "=v"((o)[0]), "=v"((o)[1]), "=v"((o)[2]), "=v"((o)[3]), "=v"((o)[4]), "=v"((o)[5]), "=v"((o)[6]), "=v"((o)[7]), "=v"((o)[8]), "=v"((o)[9]), "=v"((o)[10]), "=v"((o)[11]), "=v"((o)[12]), "=v"((o)[13]), "=v"((o)[14]), "=v"((o)[15])); break; \
    case 1: asm volatile("v_accvgpr_read_b32 %0, a16\n\t" "v_accvgpr_read_b32 %1, a17\n\t" "v_accvgpr_read_b32 %2, a18\n\t" "v_accvgpr_read_b32 %3, a19\n\t" "v_accvgpr_read_b32 %4, a20\n\t" "v_accvgpr_read_b32 %5, a21\n\t" "v_accvgpr_read_b32 %6, a22\n\t" "v_accvgpr_read_b32 %7, a23\n\t" "v_accvgpr_read_b32 %8, a24\n\t" "v_accvgpr_read_b32 %9, a25\n\t" "v_accvgpr_read_b32 %10, a26\n\t" "v_accvgpr_read_b32 %11, a27\n\t" "v_accvgpr_read_b32 %12, a28\n\t" "v_accvgpr_read_b32 %13, a29\n\t" "v_accvgpr_read_b32 %14, a30\n\t" "v_accvgpr_read_b32 %15, a31\n\t" : "=v"((o)[0]), "=v"((o)[1]), "=v"((o)[2]), "=v"((o)[3]), "=v"((o)[4]), "=v"((o)[5]), "=v"((o)[6]), "=v"((o)[7]), "=v"((o)[8]), "=v"((o)[9]), "=v"((o)[10]), "=v"((o)[11]), "=v"((o)[12]), "=v"((o)[13]), "=v"((o)[14]), "=v"((o)[15])); break; \
    case 2: asm volatile("v_accvgpr_read_b32 %0, a32\n\t" "v_accvgpr_read_b32 %1, a33\n\t" "v_accvgpr_read_b32 %2, a34\n\t" "v_accvgpr_read_b32 %3, a35\n\t" "v_accvgpr_read_b32 %4, a36\n\t" "v_accvgpr_read_b32 %5, a37\n\t" "v_accvgpr_read_b32 %6, a38\n\t" "v_accvgpr_read_b32 %7, a39\n\t" "v_accvgpr_read_b32 %8, a40\n\t" "v_accvgpr_read_b32 %9, a41\n\t" "v_accvgpr_read_b32 %10, a42\n\t" "v_accvgpr_read_b32 %11, a43\n\t" "v_accvgpr_read_b32 %12, a44\n\t" "v_accvgpr_read_b32 %13, a45\n\t" "v_accvgpr_read_b32 %14, a46\n\t" "v_accvgpr_read_b32 %15, a47\n\t" : "=v"((o)[0]), "=v"((o)[1]), "=v"((o)[2]), "=v"((o)[3]), "=v"((o)[4]), "=v"((o)[5]), "=v"((o)[6]), "=v"((o)[7]), "=v"((o)[8]), "=v"((o)[9]), "=v"((o)[10]), "=v"((o)[11]), "=v"((o)[12]), "=v"((o)[13]), "=v"((o)[14]), "=v"((o)[15])); break; \
    case 3: asm volatile("v_accvgpr_read_b32 %0, a48\n\t" "v_accvgpr_read_b32 %1, a49\n\t" "v_accvgpr_read_b32 %2, a50\n\t" "v_accvgpr_read_b32 %3, a51\n\t" "v_accvgpr_read_b32 %4, a52\n\t" "v_accvgpr_read_b32 %5, a53\n\t" "v_accvgpr_read_b32 %6, a54\n\t" "v_accvgpr_read_b32 %7, a55\n\t" "v_accvgpr_read_b32 %8, a56\n\t" "v_accvgpr_read_b32 %9, a57\n\t" "v_accvgpr_read_b32 %10, a58\n\t" "v_accvgpr_read_b32 %11, a59\n\t" "v_accvgpr_read_b32 %12, a60\n\t" "v_accvgpr_read_b32 %13, a61\n\t" "v_accvgpr_read_b32 %14, a62\n\t" "v_accvgpr_read_b32 %15, a63\n\t" : "=v"((o)[0]), "=v"((o)[1]), "=v"((o)[2]), "=v"((o)[3]), "=v"((o)[4]), "=v"((o)[5]), "=v"((o)[6]), "=v"((o)[7]), "=v"((o)[8]), "=v"((o)[9]), "=v"((o)[10]), "=v"((o)[11]), "=v"((o)[12]), "=v"((o)[13]), "=v"((o)[14]), "=v"((o)[15])); break; \
    case 4: asm volatile("v_accvgpr_read_b32 %0, a64\n\t" "v_accvgpr_read_b32 %1, a65\n\t" "v_accvgpr_read_b32 %2, a66\n\t" "v_accvgpr_read_b32 %3, a67\n\t" "v_accvgpr_read_b32 %4, a68\n\t" "v_accvgpr_read_b32 %5, a69\n\t" "v_accvgpr_read_b32 %6, a70\n\t" "v_accvgpr_read_b32 %7, a71\n\t" "v_accvgpr_read_b32 %8, a72\n\t" "v_accvgpr_read_b32 %9, a73\n\t" "v_accvgpr_read_b32 %10, a74\n\t" "v_accvgpr_read_b32 %11, a75\n\t" "v_accvgpr_read_b32 %12, a76\n\t" "v_accvgpr_read_b32 %13, a77\n\t" "v_accvgpr_read_b32 %14, a78\n\t" "v_accvgpr_read_b32 %15, a79\n\t" : "=v"((o)[0]), "=v"((o)[1]), "=v"((o)[2]), "=v"((o)[3]), "=v"((o)[4]), "=v"((o)[5]), "=v"((o)[6]), "=v"((o)[7]), "=v"((o)[8]), "=v"((o)[9]), "=v"((o)[10]), "=v"((o)[11]), "=v"((o)[12]), "=v"((o)[13]), "=v"((o)[14]), "=v"((o)[15])); break; \
    case 5: asm volatile("v_accvgpr_read_b32 %0, a80\n\t" "v_accvgpr_read_b32 %1, a81\n\t" "v_accvgpr_read_b32 %2, a82\n\t" "v_accvgpr_read_b32 %3, a83\n\t" "v_accvgpr_read_b32 %4, a84\n\t" "v_accvgpr_read_b32 %5, a85\n\t" "v_accvgpr_read_b32 %6, a86\n\t" "v_accvgpr_read_b32 %7, a87\n\t" "v_accvgpr_read_b32 %8, a88\n\t" "v_accvgpr_read_b32 %9, a89\n\t" "v_accvgpr_read_b32 %10, a90\n\t" "v_accvgpr_read_b32 %11, a91\n\t" "v_accvgpr_read_b32 %12, a92\n\t" "v_accvgpr_read_b32 %13, a93\n\t" "v_accvgpr_read_b32 %14, a94\n\t" "v_accvgpr_read_b32 %15, a95\n\t" : "=v"((o)[0]), "=v"((o)[1]), "=v"((o)[2]), "=v"((o)[3]), "=v"((o)[4]), "=v"((o)[5]), "=v"((o)[6]), "=v"((o)[7]), "=v"((o)[8]), "=v"((o)[9]), "=v"((o)[10]), "=v"((o)[11]), "=v"((o)[12]), "=v"((o)[13]), "=v"((o)[14]), "=v"((o)[15])); break; \
    case 6: asm volatile("v_accvgpr_read_b32 %0, a96\n\t" "v_accvgpr_read_b32 %1, a97\n\t" "v_accvgpr_read_b32 %2, a98\n\t" "v_accvgpr_read_b32 %3, a99\n\t" "v_accvgpr_read_b32 %4, a100\n\t" "v_accvgpr_read_b32 %5, a101\n\t" "v_accvgpr_read_b32 %6, a102\n\t" "v_accvgpr_read_b32 %7, a103\n\t" "v_accvgpr_read_b32 %8, a104\n\t" "v_accvgpr_read_b32 %9, a105\n\t" "v_accvgpr_read_b32 %10, a106\n\t" "v_accvgpr_read_b32 %11, a107\n\t" "v_accvgpr_read_b32 %12, a108\n\t" "v_accvgpr_read_b32 %13, a109\n\t" "v_accvgpr_read_b32 %14, a110\n\t" "v_accvgpr_read_b32 %15, a111\n\t" : "=v"((o)[0]), "=v"((o)[1]), "=v"((o)[2]), "=v"((o)[3]), "=v"((o)[4]), "=v"((o)[5]), "=v"((o)[6]), "=v"((o)[7]), "=v"((o)[8]), "=v"((o)[9]), "=v"((o)[10]), "=v"((o)[11]), "=v"((o)[12]), "=v"((o)[13]), "=v"((o)[14]), "=v"((o)[15])); break; \
    case 7: asm volatile("v_accvgpr_read_b32 %0, a112\n\t" "v_accvgpr_read_b32 %1, a113\n\t" "v_accvgpr_read_b32 %2, a114\n\t" "v_accvgpr_read_b32 %3, a115\n\t" "v_accvgpr_read_b32 %4, a116\n\t" "v_accvgpr_read_b32 %5, a117\n\t" "v_accvgpr_read_b32 %6, a118\n\t" "v_accvgpr_read_b32 %7, a119\n\t" "v_accvgpr_read_b32 %8, a120\n\t" "v_accvgpr_read_b32 %9, a121\n\t" "v_accvgpr_read_b32 %10, a122\n\t" "v_accvgpr_read_b32 %11, a123\n\t" "v_accvgpr_read_b32 %12, a124\n\t" "v_accvgpr_read_b32 %13, a125\n\t" "v_accvgpr_read_b32 %14, a126\n\t" "v_accvgpr_read_b32 %15, a127\n\t" : "=v"((o)[0]), "=v"((o)[1]), "=v"((o)[2]), "=v"((o)[3]), "=v"((o)[4]), "=v"((o)[5]), "=v"((o)[6]), "=v"((o)[7]), "=v"((o)[8]), "=v"((o)[9]), "=v"((o)[10]), "=v"((o)[11]), "=v"((o)[12]), "=v"((o)[13]), "=v"((o)[14]), "=v"((o)[15])); break; \
    } } while (0)
DI unsigned rfl(unsigned x) { return __builtin_amdgcn_readfirstlane(x); }
DI unsigned cvtpk(float lo, float hi) {
  f32x2 v = {lo, hi}; bf16x2 b = __builtin_convertvector(v, bf16x2); return __builtin_bit_cast(unsigned, b);
}
DI float max3f(float a, float b, float c) { float r; asm("v_max3_f32 %0, %1, %2, %3" : "=v"(r) : "v"(a), "v"(b), "v"(c)); return r; }
DI float max2f(float a, float b) { float r; asm("v_max_f32_e32 %0, %1, %2" : "=v"(r) : "v"(a), "v"(b)); return r; }
DI float fadd_s(float a, float b) { float r; asm("v_add_f32_e32 %0, %1, %2" : "=v"(r) : "v"(a), "v"(b)); return r; }
DI float fmul_s(float a, float b) { float r; asm("v_mul_f32_e32 %0, %1, %2" : "=v"(r) : "v"(a), "v"(b)); return r; }
DI float fsub_s(float a, float b) { float r; asm("v_sub_f32_e32 %0, %1, %2" : "=v"(r) : "v"(a), "v"(b)); return r; }
DI float w4_aone() { unsigned l; asm volatile("v_mbcnt_lo_u32_b32 %0, -1, 0\n\tv_mbcnt_hi_u32_b32 %0, -1, %0" : "=v"(l)); return l < 32u ? 1.f : 0.f; }
DI void negm_bcast(f32x16& negm, float aone, float neg_mhat) {
  asm volatile("s_nop 1\n\tv_mfma_f32_32x32x2_f32 %0, %1, %2, 0" : "+v"(negm) : "v"(aone), "v"(neg_mhat));
}
struct W4C {
  unsigned vkb[4], vvb, vksrc[2], vvsrc, m0k, m0v, sofk, sofv, ldsb;
  __amdgpu_buffer_rsrc_t srdK, srdV;
  __amdgpu_buffer_rsrc_t srdVm; unsigned vvs[2], sK, sV;
  __amdgpu_buffer_rsrc_t srdP; unsigned sP, m0p; int tp;
  __amdgpu_buffer_rsrc_t srdT; unsigned sT, fly;
  int T; float THR;
  f32x16 S[2][2][2];
  f32x16 NEGM[2];
  bf16x8 Vf[4][4];
  bf16x8 Pk[2][4];
  bf16x4 vlo[4][4], vup[4][4];
  float mhat[2], lsum[2], fct[2]; bool pend;
  float rs0[2], rs1[2], pmx0[2], pmx1[2], rmax[2], rboth;
};
constexpr int AO_(int db, int qb) { return 16 * (2 * db + qb); }
constexpr int AQ_(int qb, int d0) { return 128 + 32 * qb + 4 * d0; }
constexpr int AK_(int kb, int d0) { return 192 + 32 * kb + 4 * d0; }
template <int I> DI void tr_read(W4C& c, unsigned vslot) {
  constexpr int ks = I >> 3, db = (I >> 1) & 3;
  if constexpr ((I & 1) == 0) asm volatile("ds_read_b64_tr_b16 %0, %1 offset:%2" : "=v"(c.vlo[db][ks]) : "v"(c.vvb + vslot), "i"(4096 * ks + 512 * db) : "memory");
  else                        asm volatile("ds_read_b64_tr_b16 %0, %1 offset:%2" : "=v"(c.vup[db][ks]) : "v"(c.vvb + vslot), "i"(4096 * ks + 2048 + 512 * db) : "memory");
}
template <int I> DI void k_read(W4C& c, unsigned slot) {
  constexpr int kb = I >> 3, d0 = I & 7;
  asm volatile("ds_read_b128 a[%c1:%c2], %0 offset:%c3" :: "v"(c.vkb[d0 & 3] + slot), "i"(AK_(kb, d0)), "i"(AK_(kb, d0) + 3), "i"(8192 * kb + 128 * (d0 >> 2)) : "memory", W4_ACLOB_HI);
}
template <int I> DI void q_write1(const u32x4& w) {
  constexpr int aq = AQ_(I >> 3, I & 7);
  asm volatile("v_accvgpr_write_b32 a[%c4], %0\n\tv_accvgpr_write_b32 a[%c5], %1\n\tv_accvgpr_write_b32 a[%c6], %2\n\tv_accvgpr_write_b32 a[%c7], %3"
               :: "v"(w[0]), "v"(w[1]), "v"(w[2]), "v"(w[3]), "i"(aq), "i"(aq + 1), "i"(aq + 2), "i"(aq + 3) : W4_ACLOB_HI);
}
DI void q_write(const int qb, const int d0, const u32x4& w) {
  switch (8 * qb + d0) {
    case 0: q_write1<0>(w); break;   case 1: q_write1<1>(w); break;   case 2: q_write1<2>(w); break;   case 3: q_write1<3>(w); break;
    case 4: q_write1<4>(w); break;   case 5: q_write1<5>(w); break;   case 6: q_write1<6>(w); break;   case 7: q_write1<7>(w); break;
    case 8: q_write1<8>(w); break;   case 9: q_write1<9>(w); break;   case 10: q_write1<10>(w); break; case 11: q_write1<11>(w); break;
    case 12: q_write1<12>(w); break; case 13: q_write1<13>(w); break; case 14: q_write1<14>(w); break; case 15: q_write1<15>(w); break;
  }
}
template <int X, bool FIRST, int M> DI void qk_mfma(W4C& c) {
  constexpr int kb = M >> 4, d0 = (M >> 1) & 7, qb = M & 1, ak = AK_(kb, d0), aq = AQ_(qb, d0);
  if constexpr (d0 == 0) {
    if constexpr (FIRST) asm volatile("v_mfma_f32_32x32x16_bf16 %0, a[%c1:%c2], a[%c3:%c4], 0" : "=&v"(c.S[X][kb][qb]) : "i"(ak), "i"(ak + 3), "i"(aq), "i"(aq + 3) : W4_ACLOB, W4_ACLOB_HI);
    else                 asm volatile("v_mfma_f32_32x32x16_bf16 %0, a[%c2:%c3], a[%c4:%c5], %1" : "=&v"(c.S[X][kb][qb]) : "v"(c.NEGM[qb]), "i"(ak), "i"(ak + 3), "i"(aq), "i"(aq + 3) : W4_ACLOB, W4_ACLOB_HI);
  } else asm volatile("v_mfma_f32_32x32x16_bf16 %0, a[%c1:%c2], a[%c3:%c4], %0" : "+v"(c.S[X][kb][qb]) : "i"(ak), "i"(ak + 3), "i"(aq), "i"(aq + 3) : W4_ACLOB, W4_ACLOB_HI);
}
template <int M, bool ZERO> DI void pv_mfma(W4C& c) {
  constexpr int ks = M >> 3, db = (M >> 1) & 3, qb = M & 1, ao = AO_(db, qb);
  if constexpr (ZERO && ks == 0) asm volatile("v_mfma_f32_32x32x16_bf16 a[%c2:%c3], %0, %1, 0" :: "v"(c.Vf[db][ks]), "v"(c.Pk[qb][ks]), "i"(ao), "i"(ao + 15) : W4_ACLOB);
  else                         asm volatile("v_mfma_f32_32x32x16_bf16 a[%c2:%c3], %0, %1, a[%c2:%c3]" :: "v"(c.Vf[db][ks]), "v"(c.Pk[qb][ks]), "i"(ao), "i"(ao + 15) : W4_ACLOB);
}
#define S_EL(X, P) c.S[X][(P) >> 5][((P) >> 3) & 1][8 * (((P) >> 4) & 1) + ((P) & 7)]
template <int X, int P> DI void sm_step(W4C& c) {
  if constexpr (P >= 1) {
    constexpr int e = P - 1, qb = (e >> 3) & 1;
    if constexpr (e & 1) asm volatile("v_add_f32_e32 %0, %0, %1" : "+v"(c.rs1[qb]) : "v"(S_EL(X, e)));
    else                 asm volatile("v_add_f32_e32 %0, %0, %1" : "+v"(c.rs0[qb]) : "v"(S_EL(X, e)));
    if constexpr (e & 1) {
      unsigned w; asm volatile("v_cvt_pk_bf16_f32 %0, %1, %2" : "=v"(w) : "v"(S_EL(X, e - 1)), "v"(S_EL(X, e)));
      u32x4 pk = __builtin_bit_cast(u32x4, c.Pk[qb][e >> 4]); pk[(e & 7) >> 1] = w; c.Pk[qb][e >> 4] = __builtin_bit_cast(bf16x8, pk);
    }
  }
  if constexpr (P < 64) { const float r = __builtin_amdgcn_exp2f(S_EL(X, P)); asm volatile("" :: "v"(r)); S_EL(X, P) = r; }
}
DI void mx3(float& d, float a, float b, float e) { asm volatile("v_max3_f32 %0, %1, %2, %3" : "=v"(d) : "v"(a), "v"(b), "v"(e)); }
DI void mx3a(float& d, float a, float b) { asm volatile("v_max3_f32 %0, %0, %1, %2" : "+v"(d) : "v"(a), "v"(b)); }
template <int X, int J> DI void mx_lo(W4C& c) {
  constexpr int q = J / 7, k = J % 7;
  if constexpr (k == 0) mx3(c.pmx0[q], c.S[X][0][q][0], c.S[X][0][q][1], c.S[X][0][q][2]);
  else if constexpr (k == 1) mx3(c.pmx1[q], c.S[X][0][q][3], c.S[X][0][q][4], c.S[X][0][q][5]);
  else if constexpr (k & 1) mx3a(c.pmx1[q], c.S[X][0][q][2 * k + 2], c.S[X][0][q][2 * k + 3]);
  else mx3a(c.pmx0[q], c.S[X][0][q][2 * k + 2], c.S[X][0][q][2 * k + 3]);
}
template <int X, int J> DI void mx_hi(W4C& c) {
  constexpr int q = J / 9, k = J % 9;
  if constexpr (k == 8) asm volatile("v_max_f32_e32 %0, %0, %1" : "+v"(c.pmx0[q]) : "v"(c.pmx1[q]));
  else if constexpr (k & 1) mx3a(c.pmx0[q], c.S[X][1][q][2 * k], c.S[X][1][q][2 * k + 1]);
  else mx3a(c.pmx1[q], c.S[X][1][q][2 * k], c.S[X][1][q][2 * k + 1]);
}
template <int X, bool FIRST, int M> DI void gapA(W4C& c, unsigned vslot) {
  constexpr int Xp = X ^ 1;
  if constexpr (M == 16) asm volatile("s_waitcnt lgkmcnt(15)" ::: "memory");
  qk_mfma<X, FIRST, M>(c);
  if constexpr (!FIRST) {
    constexpr int base = M < 18 ? M + (M + 1) / 2 : M + 9;
    if constexpr (M <= 22) tr_read<base>(c, vslot);
    if constexpr (M < 18 && !(M & 1)) tr_read<base + 1>(c, vslot);
  }
  if constexpr (M >= 18) mx_lo<X, M - 18>(c);
  if constexpr (!FIRST) sm_step<Xp, 19 + M>(c);
  GAP();
}
template <int X, bool FIRST, int... Ms> DI void phaseA_(W4C& c, const int t, iseq<Ms...>) {
  const unsigned vslot = (unsigned)(X ^ 1) * 0x4000u;
  (gapA<X, FIRST, Ms>(c, vslot), ...);
#define W4_VLO "+v"(c.vlo[0][0]), "+v"(c.vlo[0][1]), "+v"(c.vlo[0][2]), "+v"(c.vlo[0][3]), "+v"(c.vlo[1][0]), "+v"(c.vlo[1][1]), "+v"(c.vlo[1][2]), "+v"(c.vlo[1][3]), \
               "+v"(c.vlo[2][0]), "+v"(c.vlo[2][1]), "+v"(c.vlo[2][2]), "+v"(c.vlo[2][3]), "+v"(c.vlo[3][0]), "+v"(c.vlo[3][1]), "+v"(c.vlo[3][2]), "+v"(c.vlo[3][3])
#define W4_VUP "+v"(c.vup[0][0]), "+v"(c.vup[0][1]), "+v"(c.vup[0][2]), "+v"(c.vup[0][3]), "+v"(c.vup[1][0]), "+v"(c.vup[1][1]), "+v"(c.vup[1][2]), "+v"(c.vup[1][3]), \
               "+v"(c.vup[2][0]), "+v"(c.vup[2][1]), "+v"(c.vup[2][2]), "+v"(c.vup[2][3]), "+v"(c.vup[3][0]), "+v"(c.vup[3][1]), "+v"(c.vup[3][2]), "+v"(c.vup[3][3])
  if constexpr (!FIRST) { asm volatile("s_waitcnt lgkmcnt(0)" : W4_VLO :: "memory"); asm volatile("" : W4_VUP :: "memory"); }
  GAP();
  if constexpr (!FIRST) {
    #pragma unroll
    for (int ks = 0; ks < 4; ++ks)
      #pragma unroll
      for (int db = 0; db < 4; ++db) c.Vf[db][ks] = __builtin_shufflevector(c.vlo[db][ks], c.vup[db][ks], 0, 1, 2, 3, 4, 5, 6, 7);
  }
}
template <int X, bool FIRST> DI void phaseA(W4C& c, const int t) { phaseA_<X, FIRST>(c, t, make_iseq<32>{}); }
template <int X, bool PEEL, bool ZERO1, int DK, int DV, bool KR, int TP, int M> DI void gapB(W4C& c, const int t) {
  constexpr int Xp = X ^ 1;
  if constexpr (!PEEL) pv_mfma<M, ZERO1>(c);
  if constexpr (M == 12) {
    if constexpr (TP == 2) asm volatile("s_waitcnt vmcnt(3)\n\ts_cmp_eq_u32 %0, 3\n\ts_cbranch_scc1 1f\n\ts_waitcnt vmcnt(1)\n\ts_cmp_eq_u32 %0, 1\n\ts_cbranch_scc1 1f\n\ts_waitcnt vmcnt(0)\n1:\n\ts_barrier"
                                        :: "s"(c.fly) : "memory", "scc");
    else asm volatile("s_waitcnt vmcnt(0)\n\ts_barrier" ::: "memory");
  }
  if constexpr (M == 12 && DK) asm volatile("s_add_u32 m0, %0, %1" :: "s"(c.m0k), "i"(X * 0x4000) : "m0", "scc");
  if constexpr (M == 21 && DV) asm volatile("s_add_u32 m0, %0, %1" :: "s"(c.m0v), "i"((X ^ 1) * 0x4000) : "m0", "scc");
  if constexpr (M >= 14 && M <= 20 && !(M & 1) && DK)
    asm volatile("buffer_load_dwordx4 %0, %1, %2 offen offset:%3 lds" :: "v"(c.vksrc[((M - 14) >> 1) & 1]), "s"(c.srdK), "s"(c.sK), "i"(512 * (M - 14)) : "memory");
  if constexpr (M >= 22 && M <= 28 && !(M & 1) && DV)
    asm volatile("buffer_load_dwordx4 %0, %1, %2 offen offset:%3 lds" :: "v"(c.vvs[((M - 22) >> 1) & 1]), "s"(c.srdVm), "s"(c.sV), "i"(512 * (M - 22)) : "memory");
  if constexpr (M >= 14 && M <= 29 && KR) k_read<M - 14>(c, (unsigned)(X ^ 1) * 0x4000u);
  if constexpr (M == 30 && TP == 1) {
    unsigned vt, st;
    asm volatile("s_mov_b32 %2, 0\n\ts_and_b32 %1, %3, 7\n\ts_cmp_eq_u32 %1, 2\n\ts_cbranch_scc1 2f\n\ts_cmp_lg_u32 %3, %4\n\ts_cbranch_scc1 1f\n\t"
                 "s_mov_b32 m0, %5\n\tv_mbcnt_lo_u32_b32 %0, -1, 0\n\tv_mbcnt_hi_u32_b32 %0, -1, %0\n\tv_lshlrev_b32 %0, 7, %0\n\ts_mov_b32 %2, 3\n\t"
                 "v_add_u32 %0, %7, %0\n\tbuffer_load_dword %0, %6, 0 offen lds\n\tv_add_u32 %0, 0x2000, %0\n\tbuffer_load_dword %0, %6, 0 offen lds\n\tv_add_u32 %0, 0x2000, %0\n\tbuffer_load_dword %0, %6, 0 offen lds\n\t"
                 "s_branch 1f\n2:\n\ts_mov_b32 m0, %5\n\tv_mbcnt_lo_u32_b32 %0, -1, 0\n\tv_mbcnt_hi_u32_b32 %0, -1, %0\n\tv_lshlrev_b32 %0, 7, %0\n\ts_mov_b32 %2, 1\n\t"
                 "s_add_u32 %1, %10, %9\n\tv_add_u32 %0, %1, %0\n\tbuffer_load_dword %0, %8, 0 offen lds\n1:"
                 : "=&v"(vt), "=&s"(st), "=&s"(c.fly) : "s"(t), "s"(c.tp), "s"(c.m0p), "s"(c.srdP), "s"(c.sP), "s"(c.srdT), "s"(c.sT), "s"(c.sK) : "m0", "scc", "memory");
  }
  if constexpr (M == 30) asm volatile("s_add_u32 %0, %0, %2\n\ts_add_u32 %1, %1, %2" : "+s"(c.sK), "+s"(c.sV) : "i"(KTILEB) : "scc");
  if constexpr (M >= 1 && M <= 9) { mx_hi<X, 2 * (M - 1)>(c); mx_hi<X, 2 * (M - 1) + 1>(c); }
  if constexpr (M == 10 || M == 11) {
    constexpr int q = M - 10;
    auto pr = __builtin_amdgcn_permlane32_swap(__float_as_uint(c.pmx0[q]), __float_as_uint(c.pmx0[q]), false, false);
    c.rmax[q] = max2f(__uint_as_float(pr[0]), __uint_as_float(pr[1]));
    asm volatile("" : "+v"(c.rmax[q]));
  }
  if constexpr (M == 12 && !PEEL) { asm volatile("v_max_f32_e32 %0, %1, %2" : "=v"(c.rboth) : "v"(c.rmax[0]), "v"(c.rmax[1])); }
  if constexpr (M <= 13 && !PEEL) {
    if constexpr (M < 13) sm_step<Xp, 51 + M>(c); else sm_step<Xp, 64>(c);
  }
  if constexpr (M == 13) {
    if constexpr (PEEL) {
      #pragma unroll
      for (int q = 0; q < 2; ++q) { c.mhat[q] = c.rmax[q]; c.fct[q] = 1.f;
        #pragma unroll
        for (int kb = 0; kb < 2; ++kb) { _Pragma("unroll") for (int e = 0; e < 16; ++e) c.S[X][kb][q][e] = fsub_s(c.S[X][kb][q][e], c.rmax[q]); }
        negm_bcast(c.NEGM[q], w4_aone(), -c.mhat[q]);
      }
      asm volatile("s_nop 1" ::: "memory");
    } else if (__builtin_expect(__builtin_amdgcn_ballot_w64(c.rboth > THR) != 0ull, 0)) {
      #pragma unroll
      for (int q = 0; q < 2; ++q) { const float dl = max2f(c.rmax[q], 0.f); c.mhat[q] = fadd_s(c.mhat[q], dl); c.fct[q] = __builtin_amdgcn_exp2f(-dl);
        #pragma unroll
        for (int kb = 0; kb < 2; ++kb) { _Pragma("unroll") for (int e = 0; e < 16; ++e) c.S[X][kb][q][e] = fsub_s(c.S[X][kb][q][e], dl); }
        negm_bcast(c.NEGM[q], w4_aone(), -c.mhat[q]);
        c.rs0[q] = c.rs0[q] * c.fct[q]; c.rs1[q] = c.rs1[q] * c.fct[q];
      }
      c.pend = true;
      asm volatile("s_nop 1" ::: "memory");
    }
  }
  if constexpr (M == 13) sm_step<X, 0>(c);
  if constexpr (M > 13) sm_step<X, M - 13>(c);
  GAP();
}
template <int X, bool PEEL, bool ZERO1, int DK, int DV, bool KR, int TP, int... Ms> DI void phaseB_(W4C& c, const int t, iseq<Ms...>) {
  if constexpr (!PEEL) asm volatile("s_nop 1" ::: "memory"); else asm volatile("s_nop 7\n\ts_nop 7" ::: "memory");
  GAP();
  (gapB<X, PEEL, ZERO1, DK, DV, KR, TP, Ms>(c, t), ...);
  if constexpr (!PEEL) {
    if (__builtin_expect(c.pend, 0)) {
      float t0, t1;
      asm volatile("s_nop 7\n\ts_nop 7\n\t"
        "v_accvgpr_read_b32 %0, a0\n\tv_mul_f32 %0, %2, %0\n\tv_accvgpr_write_b32 a0, %0\n\t"
        "v_accvgpr_read_b32 %1, a1\n\tv_mul_f32 %1, %2, %1\n\tv_accvgpr_write_b32 a1, %1\n\t"
        "v_accvgpr_read_b32 %0, a2\n\tv_mul_f32 %0, %2, %0\n\tv_accvgpr_write_b32 a2, %0\n\t"
        "v_accvgpr_read_b32 %1, a3\n\tv_mul_f32 %1, %2, %1\n\tv_accvgpr_write_b32 a3, %1\n\t"
        "v_accvgpr_read_b32 %0, a4\n\tv_mul_f32 %0, %2, %0\n\tv_accvgpr_write_b32 a4, %0\n\t"
        "v_accvgpr_read_b32 %1, a5\n\tv_mul_f32 %1, %2, %1\n\tv_accvgpr_write_b32 a5, %1\n\t"
        "v_accvgpr_read_b32 %0, a6\n\tv_mul_f32 %0, %2, %0\n\tv_accvgpr_write_b32 a6, %0\n\t"
        "v_accvgpr_read_b32 %1, a7\n\tv_mul_f32 %1, %2, %1\n\tv_accvgpr_write_b32 a7, %1\n\t"
        "v_accvgpr_read_b32 %0, a8\n\tv_mul_f32 %0, %2, %0\n\tv_accvgpr_write_b32 a8, %0\n\t"
        "v_accvgpr_read_b32 %1, a9\n\tv_mul_f32 %1, %2, %1\n\tv_accvgpr_write_b32 a9, %1\n\t"
        "v_accvgpr_read_b32 %0, a10\n\tv_mul_f32 %0, %2, %0\n\tv_accvgpr_write_b32 a10, %0\n\t"
        "v_accvgpr_read_b32 %1, a11\n\tv_mul_f32 %1, %2, %1\n\tv_accvgpr_write_b32 a11, %1\n\t"
        "v_accvgpr_read_b32 %0, a12\n\tv_mul_f32 %0, %2, %0\n\tv_accvgpr_write_b32 a12, %0\n\t"
        "v_accvgpr_read_b32 %1, a13\n\tv_mul_f32 %1, %2, %1\n\tv_accvgpr_write_b32 a13, %1\n\t"
        "v_accvgpr_read_b32 %0, a14\n\tv_mul_f32 %0, %2, %0\n\tv_accvgpr_write_b32 a14, %0\n\t"
        "v_accvgpr_read_b32 %1, a15\n\tv_mul_f32 %1, %2, %1\n\tv_accvgpr_write_b32 a15, %1\n\t"
        "v_accvgpr_read_b32 %0, a16\n\tv_mul_f32 %0, %3, %0\n\tv_accvgpr_write_b32 a16, %0\n\t"
        "v_accvgpr_read_b32 %1, a17\n\tv_mul_f32 %1, %3, %1\n\tv_accvgpr_write_b32 a17, %1\n\t"
        "v_accvgpr_read_b32 %0, a18\n\tv_mul_f32 %0, %3, %0\n\tv_accvgpr_write_b32 a18, %0\n\t"
        "v_accvgpr_read_b32 %1, a19\n\tv_mul_f32 %1, %3, %1\n\tv_accvgpr_write_b32 a19, %1\n\t"
        "v_accvgpr_read_b32 %0, a20\n\tv_mul_f32 %0, %3, %0\n\tv_accvgpr_write_b32 a20, %0\n\t"
        "v_accvgpr_read_b32 %1, a21\n\tv_mul_f32 %1, %3, %1\n\tv_accvgpr_write_b32 a21, %1\n\t"
        "v_accvgpr_read_b32 %0, a22\n\tv_mul_f32 %0, %3, %0\n\tv_accvgpr_write_b32 a22, %0\n\t"
        "v_accvgpr_read_b32 %1, a23\n\tv_mul_f32 %1, %3, %1\n\tv_accvgpr_write_b32 a23, %1\n\t"
        "v_accvgpr_read_b32 %0, a24\n\tv_mul_f32 %0, %3, %0\n\tv_accvgpr_write_b32 a24, %0\n\t"
        "v_accvgpr_read_b32 %1, a25\n\tv_mul_f32 %1, %3, %1\n\tv_accvgpr_write_b32 a25, %1\n\t"
        "v_accvgpr_read_b32 %0, a26\n\tv_mul_f32 %0, %3, %0\n\tv_accvgpr_write_b32 a26, %0\n\t"
        "v_accvgpr_read_b32 %1, a27\n\tv_mul_f32 %1, %3, %1\n\tv_accvgpr_write_b32 a27, %1\n\t"
        "v_accvgpr_read_b32 %0, a28\n\tv_mul_f32 %0, %3, %0\n\tv_accvgpr_write_b32 a28, %0\n\t"
        "v_accvgpr_read_b32 %1, a29\n\tv_mul_f32 %1, %3, %1\n\tv_accvgpr_write_b32 a29, %1\n\t"
        "v_accvgpr_read_b32 %0, a30\n\tv_mul_f32 %0, %3, %0\n\tv_accvgpr_write_b32 a30, %0\n\t"
        "v_accvgpr_read_b32 %1, a31\n\tv_mul_f32 %1, %3, %1\n\tv_accvgpr_write_b32 a31, %1\n\t"
        "v_accvgpr_read_b32 %0, a32\n\tv_mul_f32 %0, %2, %0\n\tv_accvgpr_write_b32 a32, %0\n\t"
        "v_accvgpr_read_b32 %1, a33\n\tv_mul_f32 %1, %2, %1\n\tv_accvgpr_write_b32 a33, %1\n\t"
        "v_accvgpr_read_b32 %0, a34\n\tv_mul_f32 %0, %2, %0\n\tv_accvgpr_write_b32 a34, %0\n\t"
        "v_accvgpr_read_b32 %1, a35\n\tv_mul_f32 %1, %2, %1\n\tv_accvgpr_write_b32 a35, %1\n\t"
        "v_accvgpr_read_b32 %0, a36\n\tv_mul_f32 %0, %2, %0\n\tv_accvgpr_write_b32 a36, %0\n\t"
        "v_accvgpr_read_b32 %1, a37\n\tv_mul_f32 %1, %2, %1\n\tv_accvgpr_write_b32 a37, %1\n\t"
        "v_accvgpr_read_b32 %0, a38\n\tv_mul_f32 %0, %2, %0\n\tv_accvgpr_write_b32 a38, %0\n\t"
        "v_accvgpr_read_b32 %1, a39\n\tv_mul_f32 %1, %2, %1\n\tv_accvgpr_write_b32 a39, %1\n\t"
        "v_accvgpr_read_b32 %0, a40\n\tv_mul_f32 %0, %2, %0\n\tv_accvgpr_write_b32 a40, %0\n\t"
        "v_accvgpr_read_b32 %1, a41\n\tv_mul_f32 %1, %2, %1\n\tv_accvgpr_write_b32 a41, %1\n\t"
        "v_accvgpr_read_b32 %0, a42\n\tv_mul_f32 %0, %2, %0\n\tv_accvgpr_write_b32 a42, %0\n\t"
        "v_accvgpr_read_b32 %1, a43\n\tv_mul_f32 %1, %2, %1\n\tv_accvgpr_write_b32 a43, %1\n\t"
        "v_accvgpr_read_b32 %0, a44\n\tv_mul_f32 %0, %2, %0\n\tv_accvgpr_write_b32 a44, %0\n\t"
        "v_accvgpr_read_b32 %1, a45\n\tv_mul_f32 %1, %2, %1\n\tv_accvgpr_write_b32 a45, %1\n\t"
        "v_accvgpr_read_b32 %0, a46\n\tv_mul_f32 %0, %2, %0\n\tv_accvgpr_write_b32 a46, %0\n\t"
        "v_accvgpr_read_b32 %1, a47\n\tv_mul_f32 %1, %2, %1\n\tv_accvgpr_write_b32 a47, %1\n\t"
        "v_accvgpr_read_b32 %0, a48\n\tv_mul_f32 %0, %3, %0\n\tv_accvgpr_write_b32 a48, %0\n\t"
        "v_accvgpr_read_b32 %1, a49\n\tv_mul_f32 %1, %3, %1\n\tv_accvgpr_write_b32 a49, %1\n\t"
        "v_accvgpr_read_b32 %0, a50\n\tv_mul_f32 %0, %3, %0\n\tv_accvgpr_write_b32 a50, %0\n\t"
        "v_accvgpr_read_b32 %1, a51\n\tv_mul_f32 %1, %3, %1\n\tv_accvgpr_write_b32 a51, %1\n\t"
        "v_accvgpr_read_b32 %0, a52\n\tv_mul_f32 %0, %3, %0\n\tv_accvgpr_write_b32 a52, %0\n\t"
        "v_accvgpr_read_b32 %1, a53\n\tv_mul_f32 %1, %3, %1\n\tv_accvgpr_write_b32 a53, %1\n\t"
        "v_accvgpr_read_b32 %0, a54\n\tv_mul_f32 %0, %3, %0\n\tv_accvgpr_write_b32 a54, %0\n\t"
        "v_accvgpr_read_b32 %1, a55\n\tv_mul_f32 %1, %3, %1\n\tv_accvgpr_write_b32 a55, %1\n\t"
        "v_accvgpr_read_b32 %0, a56\n\tv_mul_f32 %0, %3, %0\n\tv_accvgpr_write_b32 a56, %0\n\t"
        "v_accvgpr_read_b32 %1, a57\n\tv_mul_f32 %1, %3, %1\n\tv_accvgpr_write_b32 a57, %1\n\t"
        "v_accvgpr_read_b32 %0, a58\n\tv_mul_f32 %0, %3, %0\n\tv_accvgpr_write_b32 a58, %0\n\t"
        "v_accvgpr_read_b32 %1, a59\n\tv_mul_f32 %1, %3, %1\n\tv_accvgpr_write_b32 a59, %1\n\t"
        "v_accvgpr_read_b32 %0, a60\n\tv_mul_f32 %0, %3, %0\n\tv_accvgpr_write_b32 a60, %0\n\t"
        "v_accvgpr_read_b32 %1, a61\n\tv_mul_f32 %1, %3, %1\n\tv_accvgpr_write_b32 a61, %1\n\t"
        "v_accvgpr_read_b32 %0, a62\n\tv_mul_f32 %0, %3, %0\n\tv_accvgpr_write_b32 a62, %0\n\t"
        "v_accvgpr_read_b32 %1, a63\n\tv_mul_f32 %1, %3, %1\n\tv_accvgpr_write_b32 a63, %1\n\t"
        "v_accvgpr_read_b32 %0, a64\n\tv_mul_f32 %0, %2, %0\n\tv_accvgpr_write_b32 a64, %0\n\t"
        "v_accvgpr_read_b32 %1, a65\n\tv_mul_f32 %1, %2, %1\n\tv_accvgpr_write_b32 a65, %1\n\t"
        "v_accvgpr_read_b32 %0, a66\n\tv_mul_f32 %0, %2, %0\n\tv_accvgpr_write_b32 a66, %0\n\t"
        "v_accvgpr_read_b32 %1, a67\n\tv_mul_f32 %1, %2, %1\n\tv_accvgpr_write_b32 a67, %1\n\t"
        "v_accvgpr_read_b32 %0, a68\n\tv_mul_f32 %0, %2, %0\n\tv_accvgpr_write_b32 a68, %0\n\t"
        "v_accvgpr_read_b32 %1, a69\n\tv_mul_f32 %1, %2, %1\n\tv_accvgpr_write_b32 a69, %1\n\t"
        "v_accvgpr_read_b32 %0, a70\n\tv_mul_f32 %0, %2, %0\n\tv_accvgpr_write_b32 a70, %0\n\t"
        "v_accvgpr_read_b32 %1, a71\n\tv_mul_f32 %1, %2, %1\n\tv_accvgpr_write_b32 a71, %1\n\t"
        "v_accvgpr_read_b32 %0, a72\n\tv_mul_f32 %0, %2, %0\n\tv_accvgpr_write_b32 a72, %0\n\t"
        "v_accvgpr_read_b32 %1, a73\n\tv_mul_f32 %1, %2, %1\n\tv_accvgpr_write_b32 a73, %1\n\t"
        "v_accvgpr_read_b32 %0, a74\n\tv_mul_f32 %0, %2, %0\n\tv_accvgpr_write_b32 a74, %0\n\t"
        "v_accvgpr_read_b32 %1, a75\n\tv_mul_f32 %1, %2, %1\n\tv_accvgpr_write_b32 a75, %1\n\t"
        "v_accvgpr_read_b32 %0, a76\n\tv_mul_f32 %0, %2, %0\n\tv_accvgpr_write_b32 a76, %0\n\t"
        "v_accvgpr_read_b32 %1, a77\n\tv_mul_f32 %1, %2, %1\n\tv_accvgpr_write_b32 a77, %1\n\t"
        "v_accvgpr_read_b32 %0, a78\n\tv_mul_f32 %0, %2, %0\n\tv_accvgpr_write_b32 a78, %0\n\t"
        "v_accvgpr_read_b32 %1, a79\n\tv_mul_f32 %1, %2, %1\n\tv_accvgpr_write_b32 a79, %1\n\t"
        "v_accvgpr_read_b32 %0, a80\n\tv_mul_f32 %0, %3, %0\n\tv_accvgpr_write_b32 a80, %0\n\t"
        "v_accvgpr_read_b32 %1, a81\n\tv_mul_f32 %1, %3, %1\n\tv_accvgpr_write_b32 a81, %1\n\t"
        "v_accvgpr_read_b32 %0, a82\n\tv_mul_f32 %0, %3, %0\n\tv_accvgpr_write_b32 a82, %0\n\t"
        "v_accvgpr_read_b32 %1, a83\n\tv_mul_f32 %1, %3, %1\n\tv_accvgpr_write_b32 a83, %1\n\t"
        "v_accvgpr_read_b32 %0, a84\n\tv_mul_f32 %0, %3, %0\n\tv_accvgpr_write_b32 a84, %0\n\t"
        "v_accvgpr_read_b32 %1, a85\n\tv_mul_f32 %1, %3, %1\n\tv_accvgpr_write_b32 a85, %1\n\t"
        "v_accvgpr_read_b32 %0, a86\n\tv_mul_f32 %0, %3, %0\n\tv_accvgpr_write_b32 a86, %0\n\t"
        "v_accvgpr_read_b32 %1, a87\n\tv_mul_f32 %1, %3, %1\n\tv_accvgpr_write_b32 a87, %1\n\t"
        "v_accvgpr_read_b32 %0, a88\n\tv_mul_f32 %0, %3, %0\n\tv_accvgpr_write_b32 a88, %0\n\t"
        "v_accvgpr_read_b32 %1, a89\n\tv_mul_f32 %1, %3, %1\n\tv_accvgpr_write_b32 a89, %1\n\t"
        "v_accvgpr_read_b32 %0, a90\n\tv_mul_f32 %0, %3, %0\n\tv_accvgpr_write_b32 a90, %0\n\t"
        "v_accvgpr_read_b32 %1, a91\n\tv_mul_f32 %1, %3, %1\n\tv_accvgpr_write_b32 a91, %1\n\t"
        "v_accvgpr_read_b32 %0, a92\n\tv_mul_f32 %0, %3, %0\n\tv_accvgpr_write_b32 a92, %0\n\t"
        "v_accvgpr_read_b32 %1, a93\n\tv_mul_f32 %1, %3, %1\n\tv_accvgpr_write_b32 a93, %1\n\t"
        "v_accvgpr_read_b32 %0, a94\n\tv_mul_f32 %0, %3, %0\n\tv_accvgpr_write_b32 a94, %0\n\t"
        "v_accvgpr_read_b32 %1, a95\n\tv_mul_f32 %1, %3, %1\n\tv_accvgpr_write_b32 a95, %1\n\t"
        "v_accvgpr_read_b32 %0, a96\n\tv_mul_f32 %0, %2, %0\n\tv_accvgpr_write_b32 a96, %0\n\t"
        "v_accvgpr_read_b32 %1, a97\n\tv_mul_f32 %1, %2, %1\n\tv_accvgpr_write_b32 a97, %1\n\t"
        "v_accvgpr_read_b32 %0, a98\n\tv_mul_f32 %0, %2, %0\n\tv_accvgpr_write_b32 a98, %0\n\t"
        "v_accvgpr_read_b32 %1, a99\n\tv_mul_f32 %1, %2, %1\n\tv_accvgpr_write_b32 a99, %1\n\t"
        "v_accvgpr_read_b32 %0, a100\n\tv_mul_f32 %0, %2, %0\n\tv_accvgpr_write_b32 a100, %0\n\t"
        "v_accvgpr_read_b32 %1, a101\n\tv_mul_f32 %1, %2, %1\n\tv_accvgpr_write_b32 a101, %1\n\t"
        "v_accvgpr_read_b32 %0, a102\n\tv_mul_f32 %0, %2, %0\n\tv_accvgpr_write_b32 a102, %0\n\t"
        "v_accvgpr_read_b32 %1, a103\n\tv_mul_f32 %1, %2, %1\n\tv_accvgpr_write_b32 a103, %1\n\t"
        "v_accvgpr_read_b32 %0, a104\n\tv_mul_f32 %0, %2, %0\n\tv_accvgpr_write_b32 a104, %0\n\t"
        "v_accvgpr_read_b32 %1, a105\n\tv_mul_f32 %1, %2, %1\n\tv_accvgpr_write_b32 a105, %1\n\t"
        "v_accvgpr_read_b32 %0, a106\n\tv_mul_f32 %0, %2, %0\n\tv_accvgpr_write_b32 a106, %0\n\t"
        "v_accvgpr_read_b32 %1, a107\n\tv_mul_f32 %1, %2, %1\n\tv_accvgpr_write_b32 a107, %1\n\t"
        "v_accvgpr_read_b32 %0, a108\n\tv_mul_f32 %0, %2, %0\n\tv_accvgpr_write_b32 a108, %0\n\t"
        "v_accvgpr_read_b32 %1, a109\n\tv_mul_f32 %1, %2, %1\n\tv_accvgpr_write_b32 a109, %1\n\t"
        "v_accvgpr_read_b32 %0, a110\n\tv_mul_f32 %0, %2, %0\n\tv_accvgpr_write_b32 a110, %0\n\t"
        "v_accvgpr_read_b32 %1, a111\n\tv_mul_f32 %1, %2, %1\n\tv_accvgpr_write_b32 a111, %1\n\t"
        "v_accvgpr_read_b32 %0, a112\n\tv_mul_f32 %0, %3, %0\n\tv_accvgpr_write_b32 a112, %0\n\t"
        "v_accvgpr_read_b32 %1, a113\n\tv_mul_f32 %1, %3, %1\n\tv_accvgpr_write_b32 a113, %1\n\t"
        "v_accvgpr_read_b32 %0, a114\n\tv_mul_f32 %0, %3, %0\n\tv_accvgpr_write_b32 a114, %0\n\t"
        "v_accvgpr_read_b32 %1, a115\n\tv_mul_f32 %1, %3, %1\n\tv_accvgpr_write_b32 a115, %1\n\t"
        "v_accvgpr_read_b32 %0, a116\n\tv_mul_f32 %0, %3, %0\n\tv_accvgpr_write_b32 a116, %0\n\t"
        "v_accvgpr_read_b32 %1, a117\n\tv_mul_f32 %1, %3, %1\n\tv_accvgpr_write_b32 a117, %1\n\t"
        "v_accvgpr_read_b32 %0, a118\n\tv_mul_f32 %0, %3, %0\n\tv_accvgpr_write_b32 a118, %0\n\t"
        "v_accvgpr_read_b32 %1, a119\n\tv_mul_f32 %1, %3, %1\n\tv_accvgpr_write_b32 a119, %1\n\t"
        "v_accvgpr_read_b32 %0, a120\n\tv_mul_f32 %0, %3, %0\n\tv_accvgpr_write_b32 a120, %0\n\t"
        "v_accvgpr_read_b32 %1, a121\n\tv_mul_f32 %1, %3, %1\n\tv_accvgpr_write_b32 a121, %1\n\t"
        "v_accvgpr_read_b32 %0, a122\n\tv_mul_f32 %0, %3, %0\n\tv_accvgpr_write_b32 a122, %0\n\t"
        "v_accvgpr_read_b32 %1, a123\n\tv_mul_f32 %1, %3, %1\n\tv_accvgpr_write_b32 a123, %1\n\t"
        "v_accvgpr_read_b32 %0, a124\n\tv_mul_f32 %0, %3, %0\n\tv_accvgpr_write_b32 a124, %0\n\t"
        "v_accvgpr_read_b32 %1, a125\n\tv_mul_f32 %1, %3, %1\n\tv_accvgpr_write_b32 a125, %1\n\t"
        "v_accvgpr_read_b32 %0, a126\n\tv_mul_f32 %0, %3, %0\n\tv_accvgpr_write_b32 a126, %0\n\t"
        "v_accvgpr_read_b32 %1, a127\n\tv_mul_f32 %1, %3, %1\n\tv_accvgpr_write_b32 a127, %1\n\t"
        "s_nop 1" : "=&v"(t0), "=&v"(t1) : "v"(c.fct[0]), "v"(c.fct[1]) : W4_ACLOB);
      c.pend = false;
    }
  }
  asm volatile("s_waitcnt lgkmcnt(8)" ::: "memory");
  GAP();
}
template <int X, bool PEEL, bool ZERO1, int DK = 1, int DV = 1, bool KR = true, int TP = 0> DI void phaseB(W4C& c, const int t) { phaseB_<X, PEEL, ZERO1, DK, DV, KR, TP>(c, t, make_iseq<32>{}); }
template <int... Is> DI void k_reads(W4C& c, unsigned slot, iseq<Is...>) { (k_read<Is>(c, slot), ...); }
template <int... Is> DI void tr_reads(W4C& c, unsigned vslot, iseq<Is...>) { (tr_read<Is>(c, vslot), ...); }
template <int X, int P> DI void sm_drain1(W4C& c) { sm_step<X, 19 + P>(c); asm volatile("s_nop 0"); }
template <int X, int... Ps> DI void sm_drain(W4C& c, iseq<Ps...>) { (sm_drain1<X, Ps>(c), ...); }
template <bool Z, int... Ms> DI void pv_all(W4C& c, iseq<Ms...>) { (pv_mfma<Ms, Z>(c), ...); }

typedef __amdgpu_buffer_rsrc_t srd_t;
DI unsigned lane_v() { unsigned l; asm volatile("v_mbcnt_lo_u32_b32 %0, -1, 0\n\tv_mbcnt_hi_u32_b32 %0, -1, %0" : "=v"(l)); return l; }
DI void dma16(unsigned m0v, unsigned voff, srd_t srd, unsigned soff) {
  asm volatile("s_mov_b32 m0, %0\n\ts_nop 0\n\tbuffer_load_dwordx4 %1, %2, %3 offen lds" :: "s"(m0v), "v"(voff), "s"(srd), "s"(soff) : "m0", "memory");
}
DI void dma16p(unsigned m0v, unsigned voff, srd_t srd, unsigned soff) {
  asm volatile("s_nop 4\n\ts_mov_b32 m0, %0\n\ts_nop 0\n\tbuffer_load_dwordx4 %1, %2, %3 offen lds" :: "s"(m0v), "v"(voff), "s"(srd), "s"(soff) : "m0", "memory");
}
DI void touch(unsigned m0v, unsigned voff, srd_t srd, unsigned soff) {
  asm volatile("s_nop 4\n\ts_mov_b32 m0, %0\n\ts_nop 0\n\tbuffer_load_dword %1, %2, %3 offen lds" :: "s"(m0v), "v"(voff), "s"(srd), "s"(soff) : "m0", "memory");
}
template <int OFF> DI void bload16(bf16x8& d, unsigned voff, srd_t srd, unsigned soff) {
  asm volatile("buffer_load_dwordx4 %0, %1, %2, %3 offen offset:%4" : "=v"(d) : "v"(voff), "s"(srd), "s"(soff), "i"(OFF) : "memory");
}
template <int OFF> DI void trrd(bf16x4& d, unsigned addr) {
  asm volatile("ds_read_b64_tr_b16 %0, %1 offset:%2" : "=v"(d) : "v"(addr), "i"(OFF) : "memory");
}
template <bool AG, bool ZERO> DI void mfma32(f32x16& acc, const bf16x8& a, const bf16x8& b) {
  if constexpr (AG) {
    if constexpr (ZERO) asm volatile("v_mfma_f32_32x32x16_bf16 %0, %1, %2, 0" : "=a"(acc) : "v"(a), "v"(b));
    else                asm volatile("v_mfma_f32_32x32x16_bf16 %0, %1, %2, %0" : "+a"(acc) : "v"(a), "v"(b));
  } else {
    if constexpr (ZERO) asm volatile("v_mfma_f32_32x32x16_bf16 %0, %1, %2, 0" : "=&v"(acc) : "v"(a), "v"(b));
    else                asm volatile("v_mfma_f32_32x32x16_bf16 %0, %1, %2, %0" : "+v"(acc) : "v"(a), "v"(b));
  }
}
template <int N> DI void wait_vm() { asm volatile("s_waitcnt vmcnt(%0)" :: "i"(N) : "memory"); }

constexpr unsigned G_UOFF = 18432u + 128u, G_STAGE = 37120u, G_DUMMY = 0x27C00u;
constexpr unsigned Q_RING = 0x10000u, Q_STAGE = 16384u, Q_DUMMY = 0x27C00u;

template <bool GEGLU> struct GC {
  static constexpr int NB = GEGLU ? 9 : 4, XD = 4, NP = GEGLU ? 9 : 4, VMW = GEGLU ? 10 : 7;
  static constexpr unsigned STAGE = GEGLU ? G_STAGE : Q_STAGE;
  f32x16 acc[NB][2];
  bf16x4 lo[2][NB], up[2][NB];
  bf16x8 X[XD][2];
  unsigned vrd, vcur, vnext;
  unsigned vsrc[GEGLU ? 9 : 1];
  unsigned vx[2];
  srd_t srdW, srdX;
  unsigned sW, wstep, m0base, dummy, sX;
  unsigned m0A, sofA, m0B, sofB, sXn[2];
  template <int P> DI void piece(unsigned m0v, unsigned sof, bool live) {
    if constexpr (GEGLU) dma16(live ? m0v + 1024u * P : dummy, vsrc[P], srdW, sof);
    else                 dma16(live ? m0v + 1024u * P : dummy, vsrc[0], srdW, sof + 8u * 8192u * (P >> 1) + 128u * (P & 1));
  }
  template <int KS, int B, int SET> DI void rd(unsigned base) {
    if constexpr (GEGLU) { trrd<((2 * KS) * 9 + B) * 256>(lo[SET][B], base); trrd<((2 * KS + 1) * 9 + B) * 256>(up[SET][B], base); }
    else                 { trrd<4096 * KS + 512 * B>(lo[SET][B], base);      trrd<4096 * KS + 2048 + 512 * B>(up[SET][B], base); }
  }
};
constexpr float GELU_C0 = -2.0f * 1.4426950408889634f * 0.7978845608028654f, GELU_C1 = GELU_C0 * 0.044715f;
struct EP { float g0, g1, u0, u1, z0, z1; unsigned w[4]; char* Hb; unsigned kk0; float c1; W4C* c; };
template <int G> DI void epi_gap(GC<true>& a, EP& e) {
  if constexpr (G >= 16 && G <= 272) {
    constexpr int R = (G - 16) >> 2, P = R - 1, ph = (G - 16) & 3;
    if constexpr (ph == 0) {
      constexpr int b = (R & 63) >> 3, qb = (R >> 2) & 1, j = 2 * (R & 3);
      if constexpr (R == 0)
        asm volatile("v_accvgpr_read_b32 %0, %4\n\tv_accvgpr_read_b32 %1, %5\n\tv_accvgpr_read_b32 %2, %6\n\tv_accvgpr_read_b32 %3, %7"
                     : "=&v"(e.g0), "=&v"(e.g1), "=&v"(e.u0), "=&v"(e.u1) : "a"(a.acc[b][qb][j]), "a"(a.acc[b][qb][j + 1]), "a"(a.acc[b][qb][8 + j]), "a"(a.acc[b][qb][9 + j]));
      else if constexpr (R < 64)
        asm volatile("v_mul_f32_e32 %2, %2, %5\n\tv_mul_f32_e32 %3, %3, %6\n\tv_accvgpr_read_b32 %0, %7\n\tv_accvgpr_read_b32 %1, %8\n\tv_cvt_pk_bf16_f32 %4, %2, %3\n\t"
                     "v_accvgpr_read_b32 %2, %9\n\tv_accvgpr_read_b32 %3, %10"
                     : "=&v"(e.g0), "=&v"(e.g1), "+v"(e.u0), "+v"(e.u1), "=&v"(e.w[P & 3]) : "v"(e.z0), "v"(e.z1),
                       "a"(a.acc[b][qb][j]), "a"(a.acc[b][qb][j + 1]), "a"(a.acc[b][qb][8 + j]), "a"(a.acc[b][qb][9 + j]));
      else
        asm volatile("v_mul_f32_e32 %0, %0, %3\n\tv_mul_f32_e32 %1, %1, %4\n\ts_nop 0\n\tv_cvt_pk_bf16_f32 %2, %0, %1" : "+v"(e.u0), "+v"(e.u1), "=&v"(e.w[P & 3]) : "v"(e.z0), "v"(e.z1));
      if constexpr (R > 0 && (P & 3) == 3) {
        constexpr int pb = P >> 3, pq = (P >> 2) & 1;
        const unsigned kk = e.kk0 + pb;
        *(u32x4*)(e.Hb + (kk >> 1) * 4096u + pq * 2048u + (kk & 1u) * 512u) = u32x4{e.w[0], e.w[1], e.w[2], e.w[3]};
      }
    } else if constexpr (ph == 1) {
      asm volatile("v_mul_f32_e32 %0, %2, %2\n\tv_mul_f32_e32 %1, %3, %3\n\tv_fmaak_f32 %0, %4, %0, 0xc0135761\n\tv_fmaak_f32 %1, %4, %1, 0xc0135761\n\tv_mul_f32_e32 %0, %2, %0"
                   : "=&v"(e.z0), "=&v"(e.z1) : "v"(e.g0), "v"(e.g1), "v"(e.c1));
    } else if constexpr (ph == 2) {
      asm volatile("v_mul_f32_e32 %1, %5, %1\n\tv_exp_f32_e32 %0, %0\n\tv_mul_f32_e32 %2, %4, %2\n\tv_exp_f32_e32 %1, %1\n\tv_mul_f32_e32 %3, %5, %3"
                   : "+v"(e.z0), "+v"(e.z1), "+v"(e.u0), "+v"(e.u1) : "v"(e.g0), "v"(e.g1));
    } else {
      asm volatile("v_add_f32_e32 %0, 1.0, %0\n\tv_add_f32_e32 %1, 1.0, %1\n\tv_rcp_f32_e32 %0, %0\n\tv_rcp_f32_e32 %1, %1" : "+v"(e.z0), "+v"(e.z1));
    }
  }
}
template <bool GEGLU> constexpr int npc(int i) { return GEGLU ? (i == 3 ? 3 : 2) : 1; }
template <bool GEGLU> constexpr int pc0(int i) { return i == 3 ? 0 : (GEGLU ? 3 + 2 * i : 1 + i); }

template <bool GEGLU, int XB, int I, bool FIRST, bool LAST, int JX, int SE, int M> DI void ggap(GC<GEGLU>& g, bool liveA, bool liveB, GC<true>* ea, EP* e) {
  using G = GC<GEGLU>;
  constexpr int NB = G::NB, XD = G::XD, b = M >> 1, qb = M & 1, set = I & 1;
  const bf16x8 a = __builtin_shufflevector(g.lo[set][b], g.up[set][b], 0, 1, 2, 3, 4, 5, 6, 7);
  mfma32<GEGLU && (b * 2 + qb) < 16, FIRST && I == 0>(g.acc[b][qb], a, g.X[(XB + I) % XD][qb]);
  if constexpr (M < NB) {
    if constexpr (I < 3) g.template rd<I + 1, M, set ^ 1>(g.vcur);
    else if constexpr (!LAST) g.template rd<0, M, set ^ 1>(g.vnext);
  }
  if constexpr (M == NB || M == NB + 1) {
    constexpr int q = M - NB, xs = (XB + I + XD - 1) % XD, st = (I + XD - 1) & 3, sel = ((I + XD - 1) >> 2) - (XD / 4 - 1);
    if constexpr (I < JX) bload16<st * 1024>(g.X[xs][q], g.vx[q], g.srdX, g.sXn[sel]);
    else dma16(g.dummy, g.vx[q], g.srdX, g.sXn[sel]);
  }
  if constexpr (M >= NB + 2 && M < NB + 2 + npc<GEGLU>(I)) {
    constexpr int p = pc0<GEGLU>(I) + (M - NB - 2);
    if constexpr (I == 3) g.template piece<p>(g.m0B, g.sofB, liveB); else g.template piece<p>(g.m0A, g.sofA, liveA);
  }
  if constexpr (SE >= 0) epi_gap<(SE * 4 + I) * 8 + M>(*ea, *e);
  if constexpr (SE == 8 && I >= 2 && M < 6) {
    constexpr int k = (I - 2) * 6 + M, tt = k >> 2, p = k & 3;
    W4C& c = *e->c;
    if constexpr (tt == 1) dma16(c.m0v + 1024u * p, c.vvsrc, c.srdV, c.sofv + KRS * 8u * (p >> 1) + 128u * (p & 1));
    else                   dma16(c.m0k + (tt >> 1) * 0x4000u + 1024u * p, c.vksrc[p & 1], c.srdK, KTILEB * (tt >> 1) + c.sofk + KRS * 4u * p);
  }
  GAP();
}
template <bool GEGLU, int XB, int I, bool FIRST, bool LAST, int JX, int SE, int... Ms> DI void gstep(GC<GEGLU>& g, bool liveA, bool liveB, GC<true>* ea, EP* e, iseq<Ms...>) {
  using G = GC<GEGLU>;
  wait_vm<(SE == 8 && I == 3) ? G::VMW + 6 : G::VMW>();
  asm volatile("s_waitcnt lgkmcnt(0)" ::: "memory");
  if constexpr (I == 3) asm volatile("s_barrier" ::: "memory");
  #pragma unroll
  for (int b = 0; b < G::NB; ++b) asm volatile("" : "+v"(g.lo[I & 1][b]), "+v"(g.up[I & 1][b]));
  asm volatile("" : "+v"(g.X[(XB + I) % G::XD][0]), "+v"(g.X[(XB + I) % G::XD][1]));
  GAP();
  (ggap<GEGLU, XB, I, FIRST, LAST, JX, SE, Ms>(g, liveA, liveB, ea, e), ...);
}
template <bool GEGLU, int XB, bool FIRST, bool LAST, int JX = 4, int SE = -1, bool AL = false> DI void gstage(GC<GEGLU>& g, int s, int slot, GC<true>* ea = nullptr, EP* e = nullptr) {
  using G = GC<GEGLU>;
  const int sl1 = slot == 2 ? 0 : slot + 1, sl2 = slot == 0 ? 2 : slot - 1;
  g.vcur = g.vrd + (unsigned)slot * G::STAGE; g.vnext = g.vrd + (unsigned)sl1 * G::STAGE;
  const bool liveA = AL || s + 2 < 9, liveB = AL || s + 3 < 9;
  g.m0A = g.m0base + (unsigned)sl2 * G::STAGE;  g.sofA = g.sW + (unsigned)(liveA ? s + 2 : 8) * g.wstep;
  g.m0B = g.m0base + (unsigned)slot * G::STAGE; g.sofB = g.sW + (unsigned)(liveB ? s + 3 : 8) * g.wstep;
  constexpr int AH = G::XD / 4 - 1;
  g.sXn[0] = g.sX + 4096u * (unsigned)(AL || s + AH < 9 ? s + AH : 8); g.sXn[1] = g.sX + 4096u * (unsigned)(AL || s + AH + 1 < 9 ? s + AH + 1 : 8);
  constexpr auto ms = make_iseq<2 * G::NB>{};
  gstep<GEGLU, XB, 0, FIRST, LAST, JX, SE>(g, liveA, liveB, ea, e, ms);
  gstep<GEGLU, XB, 1, FIRST, LAST, JX, SE>(g, liveA, liveB, ea, e, ms);
  gstep<GEGLU, XB, 2, FIRST, LAST, JX, SE>(g, liveA, liveB, ea, e, ms);
  gstep<GEGLU, XB, 3, FIRST, LAST, JX, SE>(g, liveA, liveB, ea, e, ms);
}
template <bool GEGLU, int... Ps> DI void gpieces(GC<GEGLU>& g, int stage, iseq<Ps...>) {
  using G = GC<GEGLU>;
  asm volatile("s_nop 4" ::: "memory");
  (g.template piece<Ps>(g.m0base + (unsigned)stage * G::STAGE, g.sW + (unsigned)stage * g.wstep, true), ...);
}
template <bool GEGLU, int... Js> DI void gxloads(GC<GEGLU>& g, iseq<Js...>) {
  (bload16<((Js >> 1) & 3) * 1024>(g.X[Js >> 1][Js & 1], g.vx[Js & 1], g.srdX, g.sX + 4096u * ((Js >> 1) >> 2)), ...);
}
template <bool GEGLU, int... Bs> DI void gread0(GC<GEGLU>& g, iseq<Bs...>) { (g.template rd<0, Bs, 0>(g.vrd), ...); }
template <bool GEGLU> DI void gprologue(GC<GEGLU>& g) {
  using G = GC<GEGLU>;
  gpieces<GEGLU>(g, 0, make_iseq<G::NP>{});
  if constexpr (!GEGLU) gpieces<GEGLU>(g, 1, make_iseq<G::NP>{});
  gxloads<GEGLU>(g, make_iseq<2 * (G::XD - 1)>{});
  if constexpr (GEGLU) gpieces<GEGLU>(g, 1, make_iseq<G::NP>{});
  gpieces<GEGLU>(g, 2, make_iseq<npc<GEGLU>(3)>{});
}

template <bool GEGLU> DI void gfinish(GC<GEGLU>& g) {
  asm volatile("s_nop 7\n\ts_nop 7\n\ts_nop 7" ::: "memory");
  #pragma unroll
  for (int b = 0; b < GC<GEGLU>::NB; ++b) {
    if (GEGLU && b < 8) asm volatile("" : "+a"(g.acc[b][0]), "+a"(g.acc[b][1]));
    else       asm volatile("" : "+v"(g.acc[b][0]), "+v"(g.acc[b][1]));
  }
}
DI float geglu(float gt, float u) {
  const float z = gt * __builtin_fmaf(gt * gt, GELU_C1, GELU_C0);
  return gt * __builtin_amdgcn_rcpf(1.f + __builtin_amdgcn_exp2f(z)) * u;
}

constexpr int NS_O = 128, NS_H = 144;
DI unsigned frag_lane_off(unsigned l32, unsigned hi) { return (l32 >> 4) * 1024u + ((l32 & 15u) + 16u * hi) * 16u; }

extern "C" __global__ __launch_bounds__(256, 1)
void k1_attn(const unsigned short* __restrict__ xnF, const unsigned short* __restrict__ Kc, const unsigned short* __restrict__ Vc, const unsigned short* __restrict__ wq,
             const unsigned short* __restrict__ wg, const unsigned short* __restrict__ wu, char* __restrict__ OF, char* __restrict__ HF, char* __restrict__ SP,
             const unsigned short* __restrict__ wo, const unsigned short* __restrict__ wd) {
  extern __shared__ __attribute__((aligned(16))) char lds[];
  const unsigned wid = rfl(threadIdx.x >> 6);
  const unsigned blk = blockIdx.x, h = (blk & 7u) * 4u + ((blk >> 3) & 3u), y = blk >> 5;
  const unsigned ldsb = (unsigned)(size_t)lds;
  const srd_t srdX = __builtin_amdgcn_make_buffer_rsrc((void*)xnF, (short)0, SEQ * DM * 2, SRD_WORD3);
  const unsigned sX = (8u * y + 2u * wid) * 36864u;
  W4C c;
  c.T = NT; c.THR = THR; c.ldsb = ldsb;
  {
    GC<true> g;
    unsigned lane = lane_v(), hi = lane >> 5, l32 = lane & 31u; const unsigned i16 = lane & 15u;
    g.srdX = srdX; g.sX = sX; g.vx[0] = lane * 16u; g.vx[1] = lane * 16u + 36864u;
    g.srdW = __builtin_amdgcn_make_buffer_rsrc((void*)((wid >> 1) ? wu : wg), (short)0, DM * FF * 2, SRD_WORD3);
    g.wstep = 64u * 9216u; g.sW = (wid & 1u) * 32u * 9216u + 288u * h;
    g.m0base = ldsb + (wid >> 1) * G_UOFF + (wid & 1u) * 9216u; g.dummy = ldsb + G_DUMMY;
    #pragma unroll
    for (int p = 0; p < 9; ++p) { const unsigned s = 4u * p + (lane >> 4), k8 = s / 9u, b = s - 9u * k8; g.vsrc[p] = (8u * k8 + ((lane >> 1) & 7u)) * 9216u + 32u * b + 16u * (lane & 1u); }
    g.vrd = ldsb + (((lane >> 4) & 1u) ? G_UOFF : 0u) + 128u * hi + 32u * ((i16 >> 2) & 3u) + 8u * (i16 & 3u);
    gprologue<true>(g);
    {
      const unsigned l36 = lane < 36u ? lane : 35u;
      touch(g.dummy, (72u * y + 18u * wid + (l36 >> 1)) * 8192u + 256u * h + 128u * (l36 & 1u),
            __builtin_amdgcn_make_buffer_rsrc((void*)wq, (short)0, DM * NH * HD * 2, SRD_WORD3), 0u); }
    wait_vm<19>(); asm volatile("s_barrier" ::: "memory");
    gread0<true>(g, make_iseq<9>{});
    gstage<true, 0, true, false>(g, 0, 0);
    int slot = 1;
    for (int s = 1; s < 6; ++s) { gstage<true, 0, false, false, 4, -1, true>(g, s, slot); slot = slot == 2 ? 0 : slot + 1; }
    gstage<true, 0, false, false>(g, 6, 0);
    gstage<true, 0, false, false>(g, 7, 1);
    gstage<true, 0, false, true, 1>(g, 8, 2);
    GC<false> q;
    lane = lane_v(); hi = lane >> 5; l32 = lane & 31u;
    q.srdX = srdX; q.sX = sX; q.vx[0] = lane * 16u; q.vx[1] = lane * 16u + 36864u;
    q.srdW = __builtin_amdgcn_make_buffer_rsrc((void*)wq, (short)0, DM * NH * HD * 2, SRD_WORD3);
    q.wstep = 64u * 8192u; q.sW = 256u * h + 16u * 8192u * wid;
    q.m0base = ldsb + Q_RING + 4096u * wid; q.dummy = ldsb + Q_DUMMY;
    q.vsrc[0] = 8192u * ((lane >> 2) & 7u) + 64u * hi + 16u * (lane & 3u);
    q.vrd = ldsb + Q_RING + (((((lane & 1u) << 1) | ((lane >> 1) & 1u)) << 3) | (((lane >> 2) & 3u) << 6) | (((lane >> 4) & 1u) << 5) | (hi << 8));
    #pragma unroll
    for (int par = 0; par < 2; ++par) c.vksrc[par] = KRS * (lane >> 4) + 16u * ((lane & 15u) ^ (4u * par + (lane >> 4)));
    c.vvsrc = KRS * ((lane >> 2) & 7u) + 64u * hi + 16u * (lane & 3u);
    c.m0k = ldsb + KRING + 4096u * wid; c.m0v = ldsb + VRING + 4096u * wid;
    c.sofk = KRS * 16u * wid; c.sofv = KRS * 16u * wid;
    c.srdK = __builtin_amdgcn_make_buffer_rsrc((void*)((const char*)Kc + (size_t)h * (SEQ * 256)), (short)0, SEQ * 256, SRD_WORD3);
    c.srdV = __builtin_amdgcn_make_buffer_rsrc((void*)((const char*)Vc + (size_t)h * (SEQ * 256)), (short)0, SEQ * 256, SRD_WORD3);
    gprologue<false>(q);
    gfinish<true>(g);
    EP ep;
    {
      const unsigned le = lane_v();
      { char* fill = SP + (size_t)blk * 30720u;
        #pragma unroll
        for (int i = 0; i < 7; ++i) *(u32x4*)(fill + i * 4096u + wid * 1024u + le * 16u) = u32x4{~0u, ~0u, ~0u, ~0u};
        *(u32x2*)(fill + 28672u + wid * 512u + le * 8u) = u32x2{~0u, ~0u}; }
      ep.Hb = HF + (size_t)(4u * y + wid) * (NS_H * 4096u) + frag_lane_off(le & 31u, le >> 5); ep.kk0 = 9u * h; ep.c1 = GELU_C1; ep.c = &c;
      const unsigned kk = 9u * h + 8u;
      #pragma unroll
      for (int qb = 0; qb < 2; ++qb) {
        const f32x16 v = g.acc[8][qb];
        u32x4 w;
        #pragma unroll
        for (int e = 0; e < 4; ++e) w[e] = cvtpk(geglu(v[2 * e], v[8 + 2 * e]), geglu(v[2 * e + 1], v[9 + 2 * e]));
        *(u32x4*)(ep.Hb + (kk >> 1) * 4096u + qb * 2048u + (kk & 1u) * 512u) = w;
      }
    }
    wait_vm<21>(); asm volatile("s_barrier" ::: "memory");
    gread0<false>(q, make_iseq<4>{});
    gstage<false, 0, true, false, 4, 0>(q, 0, 0, &g, &ep);
    gstage<false, 0, false, false, 4, 1>(q, 1, 1, &g, &ep);
    gstage<false, 0, false, false, 4, 2>(q, 2, 2, &g, &ep);
    gstage<false, 0, false, false, 4, 3>(q, 3, 0, &g, &ep);
    gstage<false, 0, false, false, 4, 4>(q, 4, 1, &g, &ep);
    gstage<false, 0, false, false, 4, 5>(q, 5, 2, &g, &ep);
    gstage<false, 0, false, false, 4, 6>(q, 6, 0, &g, &ep);
    gstage<false, 0, false, false, 4, 7>(q, 7, 1, &g, &ep);
    gstage<false, 0, false, true, 1, 8>(q, 8, 2, &g, &ep);
    gfinish<false>(q);
    const float C = 0.12751743082f;
    u32x4 qw[2][8];
    #pragma unroll
    for (int qb = 0; qb < 2; ++qb)
      #pragma unroll
      for (int d0 = 0; d0 < 8; ++d0)
        #pragma unroll
        for (int e = 0; e < 4; ++e) qw[qb][d0][e] = cvtpk(q.acc[d0 >> 1][qb][8 * (d0 & 1) + 2 * e] * C, q.acc[d0 >> 1][qb][8 * (d0 & 1) + 2 * e + 1] * C);
    #pragma unroll
    for (int qb = 0; qb < 2; ++qb)
      #pragma unroll
      for (int d0 = 0; d0 < 8; ++d0) asm volatile("" : "+v"(qw[qb][d0]));
    #pragma unroll
    for (int qb = 0; qb < 2; ++qb)
      #pragma unroll
      for (int d0 = 0; d0 < 8; ++d0) q_write(qb, d0, qw[qb][d0]);
  }
  {
    const unsigned lane = lane_v(), hi = lane >> 5, l32 = lane & 31u;
    #pragma unroll
    for (int j = 0; j < 4; ++j) c.vkb[j] = ldsb + KRING + 256u * l32 + 16u * ((2u * j + hi) ^ (l32 & 7u));
    c.vvb = ldsb + VRING + (((lane & 3u) << 3) | (((lane >> 2) & 3u) << 6) | (((lane >> 4) & 1u) << 5) | (hi << 8));
    #pragma unroll
    for (int par = 0; par < 2; ++par) c.vksrc[par] = KRS * (lane >> 4) + 16u * ((lane & 15u) ^ (4u * par + (lane >> 4)));
    c.vvs[0] = KRS * ((lane >> 2) & 7u) + 64u * hi + 16u * (lane & 3u) + 1024u; c.vvs[1] = c.vvs[0] - 896u;
    c.srdVm = __builtin_amdgcn_make_buffer_rsrc((void*)((const char*)Vc + (size_t)h * (SEQ * 256) - 1024), (short)0, SEQ * 256 + 1024, SRD_WORD3);
    c.sK = rfl(2u * KTILEB + c.sofk); c.sV = rfl(KTILEB + c.sofv);
    {
      const unsigned xc = blk & 7u, role = xc >> 1, isf = role >> 1, lpw = isf ? 162u : 144u;
      c.srdP = __builtin_amdgcn_make_buffer_rsrc((void*)(isf ? wd : wo), (short)0, isf ? FF * DM * 2 : NH * HD * DM * 2, SRD_WORD3);
      c.sP = rfl(((role & 1u) * 128u + (blk >> 3) * 4u + wid) * lpw * 128u); c.tp = (int)(4u + 2u * y + 2u * ((y * 11u) >> 5)); c.m0p = ldsb + Q_DUMMY;
      c.srdT = __builtin_amdgcn_make_buffer_rsrc((void*)((const char*)(y < 4u ? Kc : Vc) + (size_t)h * (SEQ * 256)), (short)0, SEQ * 256, SRD_WORD3);
      c.sT = rfl(((y & 3u) * 4u + wid) * 8192u - c.sofk); c.fly = 0u; }
  }
  asm volatile("s_waitcnt vmcnt(0)\n\ts_barrier" ::: "memory");
  k_reads(c, 0u, make_iseq<16>{});
  asm volatile("s_waitcnt lgkmcnt(0)\n\ts_nop 1" ::: "memory");
  c.mhat[0] = c.mhat[1] = 0.f; c.rs0[0] = c.rs0[1] = c.rs1[0] = c.rs1[1] = 0.f; c.fct[0] = c.fct[1] = 1.f; c.pend = false;
  constexpr int T = NT;
  phaseA<0, true>(c, 0);  phaseB<0, true,  false>(c, 0);
  phaseA<1, false>(c, 1); phaseB<1, false, true>(c, 1);
  for (int t = 2; t < T - 2; t += 2) {
    phaseA<0, false>(c, t);           phaseB<0, false, false, 1, 1, true, 1>(c, t);
    phaseA<1, false>(c, t + 1);       phaseB<1, false, false, 1, 1, true, 2>(c, t + 1);
  }
  phaseA<0, false>(c, T - 2); phaseB<0, false, false, 0, 1, true >(c, T - 2);
  phaseA<1, false>(c, T - 1); phaseB<1, false, false, 0, 0, false>(c, T - 1);
  {
    const unsigned vslot = (unsigned)((T - 1) & 1) * 0x4000u;
    tr_reads(c, vslot, make_iseq<32>{});
    sm_drain<1>(c, make_iseq<46>{});
    #pragma unroll
    for (int qb = 0; qb < 2; ++qb) { const float rs = fadd_s(c.rs0[qb], c.rs1[qb]);
      auto pr = __builtin_amdgcn_permlane32_swap(__float_as_uint(rs), __float_as_uint(rs), false, false);
      c.lsum[qb] = fadd_s(__uint_as_float(pr[0]), __uint_as_float(pr[1])); }
    asm volatile("s_waitcnt lgkmcnt(0)" : W4_VLO :: "memory"); asm volatile("" : W4_VUP :: "memory");
    GAP();
    #pragma unroll
    for (int ks = 0; ks < 4; ++ks)
      #pragma unroll
      for (int db = 0; db < 4; ++db) c.Vf[db][ks] = __builtin_shufflevector(c.vlo[db][ks], c.vup[db][ks], 0, 1, 2, 3, 4, 5, 6, 7);
    asm volatile("s_nop 1" ::: "memory");
    pv_all<false>(c, make_iseq<32>{});
    asm volatile("s_nop 7\n\ts_nop 7" ::: "memory");
  }
  {
    const unsigned lb = lane_v();
    char* Ob = OF + (size_t)(4u * y + wid) * (NS_O * 4096u) + (size_t)(4u * h) * 4096u + frag_lane_off(lb & 31u, lb >> 5);
    #pragma unroll
    for (int qb = 0; qb < 2; ++qb) {
      const float il = __builtin_amdgcn_rcpf(c.lsum[qb]);
      #pragma unroll
      for (int db = 0; db < 4; ++db) {
        float o[16];
        W4_READ_O(db, qb, o);
        #pragma unroll
        for (int cc = 0; cc < 2; ++cc) {
          u32x4 w;
          #pragma unroll
          for (int e = 0; e < 4; ++e) w[e] = cvtpk(o[8 * cc + 2 * e] * il, o[8 * cc + 2 * e + 1] * il);
          *(u32x4*)(Ob + db * 4096u + qb * 2048u + cc * 512u) = w;
        }
      }
    }
  }
}

__global__ __launch_bounds__(512) void k0_ln(const unsigned short* __restrict__ x, char* __restrict__ xnF) {
  const unsigned lane = threadIdx.x & 63u, row = blockIdx.x * 8u + (threadIdx.x >> 6);
  float v[16];
  const bool act = lane < 36u;
  u32x4 r0 = {0, 0, 0, 0}, r1 = {0, 0, 0, 0};
  if (act) { const u32x4* p = (const u32x4*)(x + (size_t)row * DM + lane * 16u); r0 = p[0]; r1 = p[1]; }
  #pragma unroll
  for (int e = 0; e < 4; ++e) { v[2 * e] = __uint_as_float(r0[e] << 16); v[2 * e + 1] = __uint_as_float(r0[e] & 0xffff0000u);
                                v[8 + 2 * e] = __uint_as_float(r1[e] << 16); v[9 + 2 * e] = __uint_as_float(r1[e] & 0xffff0000u); }
  float s = 0.f;
  #pragma unroll
  for (int e = 0; e < 16; ++e) s += v[e];
  #pragma unroll
  for (int o = 32; o > 0; o >>= 1) s += __shfl_xor(s, o);
  const float mean = s * (1.f / DM);
  float ss = 0.f;
  #pragma unroll
  for (int e = 0; e < 16; ++e) { v[e] -= mean; ss += act ? v[e] * v[e] : 0.f; }
  #pragma unroll
  for (int o = 32; o > 0; o >>= 1) ss += __shfl_xor(ss, o);
  const float rstd = 1.f / sqrtf(ss * (1.f / DM) + 1e-5f);
  if (act) {
    char* dst = xnF + (size_t)(row >> 5) * 36864u + lane * 1024u + (row & 31u) * 16u;
    #pragma unroll
    for (int hi = 0; hi < 2; ++hi) {
      u32x4 w = {cvtpk(v[4 * hi] * rstd, v[4 * hi + 1] * rstd), cvtpk(v[4 * hi + 2] * rstd, v[4 * hi + 3] * rstd),
                 cvtpk(v[8 + 4 * hi] * rstd, v[9 + 4 * hi] * rstd), cvtpk(v[10 + 4 * hi] * rstd, v[11 + 4 * hi] * rstd)};
      *(u32x4*)(dst + hi * 512) = w;
    }
  }
}
typedef __attribute__((address_space(1))) unsigned gu32;
constexpr unsigned K2_XT = 4u * 3u * 4u * 2560u;
constexpr unsigned K2_WAVE_LDS = 40960u, K2_SLOT = 10240u, K2_ROWB = 592u;
struct K2C {
  f32x4 acc[9][4];
  bf16x4 lo[2][9], up[2][9];
  bf16x8 X[4][4];
  unsigned vrd[2], vsrc[5], vx, m0, sW, sX, sx, sw;
  srd_t srdW, srdX;
  int last;
};
template <bool ZERO> DI void mfma16(f32x4& acc, const bf16x8& a, const bf16x8& b) {
  if constexpr (ZERO) asm volatile("v_mfma_f32_16x16x32_bf16 %0, %1, %2, 0" : "=a"(acc) : "v"(a), "v"(b));
  else                asm volatile("v_mfma_f32_16x16x32_bf16 %0, %1, %2, %0" : "+a"(acc) : "v"(a), "v"(b));
}
template <int SLOT, int SET, int R> DI void k2rd1(K2C& g) {
  constexpr int b = R >> 1, off = SLOT * 10240 + (R & 1) * 2560 + (b >> 1) * 512;
  if constexpr (R & 1) trrd<off>(g.up[SET][b], g.vrd[b & 1]); else trrd<off>(g.lo[SET][b], g.vrd[b & 1]);
}
template <int I, bool FIRST, int M> DI void k2gap(K2C& g, int st) {
  constexpr int b = M >> 2, mb = M & 3, set = I & 1;
  const bf16x8 a = __builtin_shufflevector(g.lo[set][b], g.up[set][b], 0, 1, 2, 3, 4, 5, 6, 7);
  mfma16<FIRST>(g.acc[b][mb], a, g.X[I][mb]);
  if constexpr (M < 18) k2rd1<(I + 1) & 3, set ^ 1, M>(g);
  if constexpr (M == 1) {
    const int s3 = st + 3 < g.last ? st + 3 : g.last, s4 = st + 4 < g.last ? st + 4 : g.last;
    g.sx = g.sX + 4096u * (unsigned)s3; g.sw = g.sW + 36864u * (unsigned)s4;
    asm volatile("" : "+s"(g.sx), "+s"(g.sw));
  }
  if constexpr (M >= 18 && M < 26 && !(M & 1)) bload16<((M - 18) >> 1) * 1024>(g.X[(I + 3) & 3][(M - 18) >> 1], g.vx, g.srdX, g.sx);
  if constexpr (M == 25) asm volatile("s_add_u32 m0, %0, %1" :: "s"(g.m0), "i"(I * K2_SLOT) : "m0", "scc");
  if constexpr (M == 33) asm volatile("s_add_u32 m0, %0, %1" :: "s"(g.m0), "i"(I * K2_SLOT + 4096) : "m0", "scc");
  if constexpr (M >= 26 && !(M & 1))
    asm volatile("buffer_load_dwordx4 %0, %1, %2 offen offset:%3 lds" :: "v"(g.vsrc[(M - 26) >> 1]), "s"(g.srdW), "s"(g.sw), "i"((((M - 26) >> 1) & 3) * 1024) : "memory");
  GAP();
}
template <int I, bool FIRST, int... Ms> DI void k2step(K2C& g, int st, iseq<Ms...>) {
  wait_vm<18>();
  asm volatile("s_waitcnt lgkmcnt(0)\n\ts_barrier" ::: "memory");
  #pragma unroll
  for (int b = 0; b < 9; ++b) asm volatile("" : "+v"(g.lo[I & 1][b]), "+v"(g.up[I & 1][b]));
  asm volatile("" : "+v"(g.X[I][0]), "+v"(g.X[I][1]), "+v"(g.X[I][2]), "+v"(g.X[I][3]));
  GAP();
  (k2gap<I, FIRST, Ms>(g, st), ...);
}
template <int P> DI void k2w1(K2C& g, int st) {
  asm volatile("s_nop 4\n\ts_mov_b32 m0, %0\n\ts_nop 0\n\tbuffer_load_dwordx4 %1, %2, %3 offen offset:%4 lds"
               :: "s"(g.m0 + st * K2_SLOT + (P >> 2) * 4096u), "v"(g.vsrc[P]), "s"(g.srdW), "s"(g.sW + 36864u * (unsigned)st), "i"((P & 3) * 1024) : "m0", "memory");
}
template <int... Ps> DI void k2w(K2C& g, int st, iseq<Ps...>) { (k2w1<Ps>(g, st), ...); }
template <int ST, int... Ps> DI void k2x(K2C& g, iseq<Ps...>) { (bload16<Ps * 1024>(g.X[ST][Ps], g.vx, g.srdX, g.sX + 4096u * ST), ...); }
template <int... Rs> DI void k2rd0(K2C& g, iseq<Rs...>) { (k2rd1<0, 0, Rs>(g), ...); }

extern "C" __global__ __launch_bounds__(256, 1)
void k2_out(const char* __restrict__ OF, const char* __restrict__ HF, const unsigned short* __restrict__ wo, const unsigned short* __restrict__ wd,
            const unsigned short* __restrict__ x, unsigned short* __restrict__ out, char* __restrict__ SP) {
  extern __shared__ __attribute__((aligned(16))) char lds[];
  const unsigned tid = threadIdx.x, lane = tid & 63u;
  const unsigned wid = rfl(tid >> 6), mh = wid & 1u, kh = wid >> 1;
  const unsigned blk = blockIdx.x, xc = blk & 7u, idx = blk >> 3;
  const unsigned role = xc >> 1; const bool isf = role >= 2u;
  const unsigned m128 = (xc & 1u) * 8u + (idx >> 2), nt = idx & 3u, tile = m128 * 4u + nt;
  const unsigned ldsb = (unsigned)(size_t)lds;
  const int nst = isf ? 36 : 32;
  const unsigned kq = 2u * (role & 1u) + kh;
  K2C g;
  g.last = nst - 1;
  g.srdX = __builtin_amdgcn_make_buffer_rsrc((void*)(isf ? HF : OF), (short)0, isf ? SEQ * FF * 2 : SEQ * NH * HD * 2, SRD_WORD3);
  g.srdW = __builtin_amdgcn_make_buffer_rsrc((void*)((const char*)(isf ? wd : wo) - 3072), (short)0, (isf ? FF * DM * 2 : NH * HD * DM * 2) + 3072, SRD_WORD3);
  g.sX = ((m128 * 2u + mh) * 4u + kq) * (unsigned)nst * 4096u;
  const unsigned cstart = (288u * nt) & ~63u, shift = (288u * nt) & 63u;
  g.sW = kq * (unsigned)nst * 36864u + cstart;
  g.vx = lane * 16u;
  g.m0 = ldsb + kh * K2_WAVE_LDS + mh * 5120u;
  #pragma unroll
  for (int p = 0; p < 5; ++p) {
    const unsigned s = 10u * mh + 2u * p + (lane >> 5), k8 = s / 5u, cb = s - 5u * k8, row = (lane >> 2) & 7u;
    g.vsrc[p] = (8u * k8 + row) * 1152u + 64u * cb + 16u * ((lane & 3u) ^ ((row >> 2) << 1)) + 3072u - 1024u * (p & 3);
  }
  { const unsigned i16 = lane & 15u, g4 = lane >> 4, hi = g4 & 1u;
    const unsigned base = ldsb + kh * K2_WAVE_LDS + (g4 >> 1) * 5120u + (4u * hi + (i16 >> 2)) * 64u + 8u * (i16 & 3u);
    const unsigned h0 = base + 32u * hi, h1 = base + 32u * (1u ^ hi);
    g.vrd[0] = shift ? h1 : h0; g.vrd[1] = shift ? h0 + 512u : h1; }
  constexpr auto p5 = make_iseq<5>{}; constexpr auto p4 = make_iseq<4>{};
  k2w(g, 0, p5); k2x<0>(g, p4); k2w(g, 1, p5); k2x<1>(g, p4); k2w(g, 2, p5); k2x<2>(g, p4); k2w(g, 3, p5);
  wait_vm<27>();
  asm volatile("s_barrier" ::: "memory");
  k2rd0(g, make_iseq<18>{});
  constexpr auto ms = make_iseq<36>{};
  k2step<0, true>(g, 0, ms); k2step<1, false>(g, 1, ms); k2step<2, false>(g, 2, ms); k2step<3, false>(g, 3, ms);
  for (int st = 4; st < nst; st += 4) { k2step<0, false>(g, st, ms); k2step<1, false>(g, st + 1, ms); k2step<2, false>(g, st + 2, ms); k2step<3, false>(g, st + 3, ms); }
  asm volatile("s_waitcnt vmcnt(0) lgkmcnt(0)\n\ts_nop 7\n\ts_nop 7\n\ts_barrier" ::: "memory");
  #pragma unroll
  for (int j = 0; j < 4; ++j) asm volatile("" :: "v"(g.X[j][0]), "v"(g.X[j][1]), "v"(g.X[j][2]), "v"(g.X[j][3]));
  #pragma unroll
  for (int b = 0; b < 9; ++b) asm volatile("" : "+a"(g.acc[b][0]), "+a"(g.acc[b][1]), "+a"(g.acc[b][2]), "+a"(g.acc[b][3]));
  {
    char* mine = lds + wid * 20480u + lane * 16u;
    const char* theirs = lds + (wid ^ 2u) * 20480u + lane * 16u;
    if (kh == 0) { _Pragma("unroll") for (int b = 5; b < 9; ++b) _Pragma("unroll") for (int mb = 0; mb < 4; ++mb) *(f32x4*)(mine + ((b - 5) * 4 + mb) * 1024) = g.acc[b][mb]; }
    else         { _Pragma("unroll") for (int b = 0; b < 5; ++b) _Pragma("unroll") for (int mb = 0; mb < 4; ++mb) *(f32x4*)(mine + (b * 4 + mb) * 1024) = g.acc[b][mb]; }
    __syncthreads();
    f32x4 sum[5][4];
    if (kh == 0) {
      #pragma unroll
      for (int j = 0; j < 5; ++j)
        #pragma unroll
        for (int mb = 0; mb < 4; ++mb) sum[j][mb] = g.acc[j][mb] + *(const f32x4*)(theirs + (j * 4 + mb) * 1024);
    } else {
      #pragma unroll
      for (int j = 0; j < 4; ++j)
        #pragma unroll
        for (int mb = 0; mb < 4; ++mb) sum[j][mb] = *(const f32x4*)(theirs + (j * 4 + mb) * 1024) + g.acc[5 + j][mb];
      #pragma unroll
      for (int mb = 0; mb < 4; ++mb) sum[4][mb] = f32x4{0.f, 0.f, 0.f, 0.f};
    }
    const srd_t srdS = __builtin_amdgcn_make_buffer_rsrc((void*)(SP + (size_t)tile * K2_XT), (short)0, K2_XT, SRD_WORD3);
    f32x4 own[5];
    #pragma unroll
    for (int mb = 0; mb < 4; ++mb) {
      if ((unsigned)mb == role) {
        #pragma unroll
        for (int j = 0; j < 5; ++j) { own[j] = sum[j][mb]; asm volatile("" : "+v"(own[j])); }
      } else {
        unsigned w[10];
        #pragma unroll
        for (int j = 0; j < 5; ++j) { w[2 * j] = min(cvtpk(sum[j][mb][0], sum[j][mb][1]), 0xfffffffeu); w[2 * j + 1] = min(cvtpk(sum[j][mb][2], sum[j][mb][3]), 0xfffffffeu); }
        const unsigned so = ((mb * 3u + ((unsigned)mb ^ role) - 1u) * 4u + wid) * 2560u;
        __builtin_amdgcn_raw_buffer_store_b128(u32x4{w[0], w[1], w[2], w[3]}, srdS, lane * 16u, so, 16);
        __builtin_amdgcn_raw_buffer_store_b128(u32x4{w[4], w[5], w[6], w[7]}, srdS, lane * 16u, so + 1024u, 16);
        if (kh == 0) __builtin_amdgcn_raw_buffer_store_b64(u32x2{w[8], w[9]}, srdS, lane * 8u, so + 2048u, 16);
      }
    }
    const unsigned b0 = 5u * kh;
    const unsigned row = m128 * 128u + mh * 64u + role * 16u + (lane & 15u), col0 = nt * 144u + b0 * 16u + (lane >> 4) * 4u;
    u32x2 xv[5];
    #pragma unroll
    for (int j = 0; j < 5; ++j) { xv[j] = u32x2{0u, 0u}; if (j < 4 || kh == 0) xv[j] = *(const u32x2*)(x + (size_t)row * DM + col0 + 16u * j); }
    u32x4 ra[3], rb[3]; u32x2 rc[3];
    #pragma unroll
    for (int d = 0; d < 3; ++d) { ra[d] = rb[d] = u32x4{0u, 0u, 0u, 0u}; rc[d] = u32x2{0u, 0u}; }
    unsigned pend = 7u;
    for (unsigned spins = 0; pend != 0u && spins < (1u << 20); ++spins) {
      asm volatile("" ::: "memory");
      #pragma unroll
      for (int d = 0; d < 3; ++d)
        if (pend & (1u << d)) {
          const unsigned so = ((role * 3u + d) * 4u + wid) * 2560u;
          ra[d] = __builtin_amdgcn_raw_buffer_load_b128(srdS, lane * 16u, so, 16);
          rb[d] = __builtin_amdgcn_raw_buffer_load_b128(srdS, lane * 16u, so + 1024u, 16);
          if (kh == 0) rc[d] = __builtin_amdgcn_raw_buffer_load_b64(srdS, lane * 8u, so + 2048u, 16);
        }
      #pragma unroll
      for (int d = 0; d < 3; ++d)
        if (pend & (1u << d)) {
          const unsigned m = max(max(max(max(ra[d][0], ra[d][1]), max(ra[d][2], ra[d][3])), max(max(rb[d][0], rb[d][1]), max(rb[d][2], rb[d][3]))), max(rc[d][0], rc[d][1]));
          if (__builtin_amdgcn_ballot_w64(m == 0xffffffffu) == 0ull) pend &= ~(1u << d);
        }
    }
    #pragma unroll
    for (int j = 0; j < 5; ++j)
      if (j < 4 || kh == 0) {
        float o[4];
        #pragma unroll
        for (int e = 0; e < 4; ++e) {
          float p[3];
          #pragma unroll
          for (int d = 0; d < 3; ++d) {
            const unsigned w = j < 2 ? ra[d][2 * j + (e >> 1)] : j < 4 ? rb[d][2 * (j - 2) + (e >> 1)] : rc[d][e >> 1];
            p[d] = __uint_as_float((e & 1) ? (w & 0xffff0000u) : (w << 16));
          }
          const float same = own[j][e] + p[0], other = p[1] + p[2];
          const float sv = isf ? other : same, f = isf ? same : other;
          const float xe = __uint_as_float((e & 1) ? (xv[j][e >> 1] & 0xffff0000u) : (xv[j][e >> 1] << 16));
          o[e] = xe + sv * (f * __builtin_amdgcn_rcpf(1.f + __builtin_amdgcn_exp2f(-1.4426950408889634f * f)));
        }
        *(u32x2*)(out + (size_t)row * DM + col0 + 16u * j) = u32x2{cvtpk(o[0], o[1]), cvtpk(o[2], o[3])};
      }
  }
}

constexpr size_t WS_XN = 0, WS_H = 4u << 20, WS_O = WS_H + (size_t)SEQ * FF * 2, WS_S = WS_O + (size_t)SEQ * NH * HD * 2, WS_END = WS_S + (size_t)64 * K2_XT;
extern "C" void kernel_launch(void* const* d_in, const int* in_sizes, int n_in, void* d_out, int out_size, void* d_ws, size_t ws_size, hipStream_t stream) {
  (void)in_sizes; (void)n_in; (void)out_size; (void)ws_size;
  (void)hipFuncSetAttribute((const void*)k1_attn, hipFuncAttributeMaxDynamicSharedMemorySize, 163840);
  (void)hipFuncSetAttribute((const void*)k2_out, hipFuncAttributeMaxDynamicSharedMemorySize, 163840);
  char* ws = (char*)d_ws;
  const unsigned short* x = (const unsigned short*)d_in[0];
  hipLaunchKernelGGL(k0_ln, dim3(256), dim3(512), 0, stream, x, ws + WS_XN);
  hipLaunchKernelGGL(k1_attn, dim3(256), dim3(256), 163840, stream, (const unsigned short*)(ws + WS_XN), (const unsigned short*)d_in[1], (const unsigned short*)d_in[2],
                     (const unsigned short*)d_in[3], (const unsigned short*)d_in[6], (const unsigned short*)d_in[7], ws + WS_O, ws + WS_H, ws + WS_S,
                     (const unsigned short*)d_in[4], (const unsigned short*)d_in[8]);
  hipLaunchKernelGGL(k2_out, dim3(256), dim3(256), 163840, stream, (const char*)(ws + WS_O), (const char*)(ws + WS_H), (const unsigned short*)d_in[4], (const unsigned short*)d_in[8],
                     x, (unsigned short*)d_out, ws + WS_S);
}
```
